```python
import torch

def _softmax(x, axis=-1):
    return torch.softmax(x, dim=axis)
def _gelu(x):
    return torch.nn.functional.gelu(x, approximate="tanh")
def _silu(x):
    return torch.nn.functional.silu(x)
def _sigmoid(x):
    return torch.sigmoid(x)
def _layernorm(x, axis=-1, eps=1e-5):
    m = x.mean(dim=axis, keepdim=True)
    v = x.var(dim=axis, keepdim=True, unbiased=False)
    return (x - m) / torch.sqrt(v + eps)
def _scatter_add(tgt, idx, src, axis=0):
    return tgt.clone().index_add_(axis, idx, src)
def _segment_sum(src, seg, n):
    out = torch.zeros((n,) + src.shape[1:], dtype=src.dtype)
    return out.index_add_(0, seg, src)
def _pad(x, pw):
    flat = tuple(v for pair in reversed(pw) for v in pair)
    return torch.nn.functional.pad(x, flat)

kernel_name = 'gated_mix__ssm_conv_h3__geglu_ffn__prompt'

def setup_inputs():

    _w = (lambda a: (lambda t: t.to(torch.bfloat16) if t.is_floating_point() else t)(torch.as_tensor(a)))
    def _A_setup():
        B, S, D = 1, 2048, 512
        ks = 4
        return {
            "q": _w((lambda *s: torch.randn(*s, dtype=torch.bfloat16) * 0.05)(B, S, D)),
            "k": _w((lambda *s: torch.randn(*s, dtype=torch.bfloat16) * 0.2)(B, S, D)),
            "v": _w((lambda *s: torch.randn(*s, dtype=torch.bfloat16) * 0.2)(B, S, D)),
            "shift_k": _w((lambda *s: torch.randn(*s, dtype=torch.bfloat16) * 0.2)(D, ks) * 0.5),
            "ssm_k": _w((lambda *s: torch.randn(*s, dtype=torch.bfloat16) * 0.8)(D, S) * (1.0 / S ** 0.5)),
            "ssm_D": _w((lambda *s: torch.randn(*s, dtype=torch.bfloat16) * 0.2)(D)),
        }
    _A_in = _A_setup()

    def _B_setup():
        B, S, D, F = 1, 2048, 512, 1376
        return {
            "x": _w((lambda *s: torch.randn(*s, dtype=torch.bfloat16) * 0.05)(B, S, D)),
            "w_gate": _w((lambda *s: torch.randn(*s, dtype=torch.bfloat16) * 0.05)(D, F)),
            "w_up": _w((lambda *s: torch.randn(*s, dtype=torch.bfloat16) * 0.05)(D, F)),
            "w_down": _w((lambda *s: torch.randn(*s, dtype=torch.bfloat16) * 0.05)(F, D)),
        }
    _B_in = _B_setup()

    return {
        "A_q": _A_in["q"],
        "A_k": _A_in["k"],
        "A_v": _A_in["v"],
        "A_shift_k": _A_in["shift_k"],
        "A_ssm_k": _A_in["ssm_k"],
        "A_ssm_D": _A_in["ssm_D"],
        "B_x": _B_in["x"],
        "B_w_gate": _B_in["w_gate"],
        "B_w_up": _B_in["w_up"],
        "B_w_down": _B_in["w_down"],
        "gate_w": _w((lambda *s: torch.randn(*s, dtype=torch.bfloat16) * 0.05)(512) * 16.0),
    }

def reference(A_q, A_k, A_v, A_shift_k, A_ssm_k, A_ssm_D, B_x, B_w_gate, B_w_up, B_w_down, gate_w):
    def _A_ref(q, k, v, shift_k, ssm_k, ssm_D):
        B, S, D = q.shape
        ks = shift_k.shape[-1]
        k_pad = _pad(k, ((0, 0), (ks - 1, 0), (0, 0)))
        k_shift = torch.zeros_like(k)
        for t in range(ks):
            k_shift = k_shift + k_pad[:, t:t + S, :] * shift_k[:, ks - 1 - t][None, None, :]
        kv = k_shift * v
        L = 2 * S
        kv_pad = _pad(kv, ((0, 0), (0, S), (0, 0)))
        kf_pad = _pad(ssm_k, ((0, 0), (0, S)))
        kv_f = (lambda x, axes=(-2,-1): torch.fft.rfft2(x.float(), dim=axes))(kv_pad[:, None, :, :], axes=(1, 2))
        kf_f = (lambda x, axes=(-2,-1): torch.fft.rfft2(x.float(), dim=axes))(kf_pad[:, None, :], axes=(1, 2))
        kf_f = (lambda x, *d: x.permute(*d))(kf_f, 1, 2, 0)
        y_f = kv_f * kf_f[None, :, :, :]
        y = (lambda x, s, axes=(-2,-1): torch.fft.irfft2(x, s=s, dim=axes).to(torch.bfloat16))(y_f, (1, L), axes=(1, 2))[:, 0, :S, :]
        y = y + kv * ssm_D[None, None, :]
        return q * y

    def _B_ref(x, w_gate, w_up, w_down):
        return (_gelu(x @ w_gate) * (x @ w_up)) @ w_down

    x = A_q
    g = _sigmoid((x * gate_w).sum(-1, keepdims=True))
    a = _A_ref(x, A_k, A_v, A_shift_k, A_ssm_k, A_ssm_D)
    b = _B_ref(x, B_w_gate, B_w_up, B_w_down)
    return g * a + (1 - g) * b
```

```cpp
#include <hip/hip_runtime.h>
#include <hip/hip_bf16.h>
#include <stdint.h>

typedef unsigned short u16;
__device__ __forceinline__ float bf2f(u16 x) { return __uint_as_float(((unsigned)x) << 16); }
__device__ __forceinline__ u16 f2bf(float f) {
    __hip_bfloat16 b = __float2bfloat16(f); return *reinterpret_cast<u16*>(&b); }
__device__ __forceinline__ float rbf(float f) { return bf2f(f2bf(f)); }

constexpr int S = 2048, D = 512, F = 1376;

__global__ void prep_kernel(const u16* k, const u16* v, const u16* shift_k, u16* kvT) {
    int idx = blockIdx.x * blockDim.x + threadIdx.x;
    int s = idx / D, d = idx % D;
    float acc = 0.f;
    for (int t = 0; t < 4; ++t) {
        int ss = s + t - 3;
        float kk = ss >= 0 ? bf2f(k[ss * D + d]) : 0.f;
        float p = rbf(kk * bf2f(shift_k[d * 4 + 3 - t]));
        acc = rbf(acc + p);
    }
    float kv = rbf(acc * bf2f(v[s * D + d]));
    kvT[d * S + s] = f2bf(kv);
}

__global__ void gate_kernel(const u16* q, const u16* gw, float* g) {
    int s = blockIdx.x;
    int lane = threadIdx.x;
    float acc = 0.f;
    for (int d = lane; d < D; d += 64) acc += rbf(bf2f(q[s * D + d]) * bf2f(gw[d]));
    for (int m = 32; m >= 1; m >>= 1) acc += __shfl_xor(acc, m);
    if (lane == 0) {
        float z = rbf(acc);
        g[s] = rbf(1.f / (1.f + __expf(-z)));
    }
}

__global__ void conv_kernel(const u16* kvT, const u16* ssm_k, const u16* ssm_D, u16* y2) {
    __shared__ float u[S], h[S];
    int d = blockIdx.x;
    for (int i = threadIdx.x; i < S; i += blockDim.x) { u[i] = bf2f(kvT[d * S + i]); h[i] = bf2f(ssm_k[d * S + i]); }
    __syncthreads();
    float Dd = bf2f(ssm_D[d]);
    for (int s = threadIdx.x; s < S; s += blockDim.x) {
        float acc = 0.f;
        for (int t = 0; t <= s; ++t) acc += u[t] * h[s - t];
        float y = rbf(acc);
        y = rbf(y + rbf(u[s] * Dd));
        y2[s * D + d] = f2bf(y);
    }
}

__device__ __forceinline__ float gelu_tanh(float x) {
    float inner = 0.7978845608028654f * (x + 0.044715f * x * x * x);
    return 0.5f * x * (1.f + tanhf(inner));
}

__global__ void gemm1_kernel(const u16* x, const u16* wg, const u16* wu, u16* H) {
    int f = blockIdx.x * 64 + threadIdx.x % 64;
    int s = blockIdx.y * 4 + threadIdx.x / 64;
    if (f >= F) return;
    float ag = 0.f, au = 0.f;
    for (int k = 0; k < D; ++k) {
        float xv = bf2f(x[s * D + k]);
        ag += xv * bf2f(wg[k * F + f]);
        au += xv * bf2f(wu[k * F + f]);
    }
    float gg = rbf(gelu_tanh(rbf(ag)));
    H[s * F + f] = f2bf(gg * rbf(au));
}

__global__ void gemm2_kernel(const u16* H, const u16* wd, const u16* q, const u16* y2, const float* g, u16* out) {
    int d = blockIdx.x * 64 + threadIdx.x % 64;
    int s = blockIdx.y * 4 + threadIdx.x / 64;
    float acc = 0.f;
    for (int k = 0; k < F; ++k) acc += bf2f(H[s * F + k]) * bf2f(wd[k * D + d]);
    float b = rbf(acc);
    float a = rbf(bf2f(q[s * D + d]) * bf2f(y2[s * D + d]));
    float gg = g[s];
    float o = rbf(rbf(gg * a) + rbf(rbf(1.f - gg) * b));
    out[s * D + d] = f2bf(o);
}

extern "C" void kernel_launch(void* const* d_in, const int* in_sizes, int n_in,
                              void* d_out, int out_size, void* d_ws, size_t ws_size,
                              hipStream_t stream) {
    const u16* q = (const u16*)d_in[0];
    const u16* k = (const u16*)d_in[1];
    const u16* v = (const u16*)d_in[2];
    const u16* shift_k = (const u16*)d_in[3];
    const u16* ssm_k = (const u16*)d_in[4];
    const u16* ssm_D = (const u16*)d_in[5];
    const u16* wg = (const u16*)d_in[7];
    const u16* wu = (const u16*)d_in[8];
    const u16* wd = (const u16*)d_in[9];
    const u16* gw = (const u16*)d_in[10];
    u16* out = (u16*)d_out;
    char* ws = (char*)d_ws;
    u16* kvT = (u16*)ws; ws += S * D * 2;
    u16* y2 = (u16*)ws; ws += S * D * 2;
    u16* H = (u16*)ws; ws += S * F * 2;
    float* g = (float*)ws;
    prep_kernel<<<S * D / 256, 256, 0, stream>>>(k, v, shift_k, kvT);
    gate_kernel<<<S, 64, 0, stream>>>(q, gw, g);
    conv_kernel<<<D, 256, 0, stream>>>(kvT, ssm_k, ssm_D, y2);
    gemm1_kernel<<<dim3((F + 63) / 64, S / 4), 256, 0, stream>>>(q, wg, wu, H);
    gemm2_kernel<<<dim3(D / 64, S / 4), 256, 0, stream>>>(H, wd, q, y2, g, out);
}
```

```cpp
#include <hip/hip_runtime.h>
#include <hip/hip_bf16.h>
#include <stdint.h>

typedef unsigned short u16;
typedef short s16x4 __attribute__((ext_vector_type(4)));
typedef short s16x8 __attribute__((ext_vector_type(8)));
typedef __bf16 bf16x8 __attribute__((ext_vector_type(8)));
typedef __bf16 bf16x2 __attribute__((ext_vector_type(2)));
typedef float f32x2 __attribute__((ext_vector_type(2)));
typedef float f32x4 __attribute__((ext_vector_type(4)));
typedef float f32x16 __attribute__((ext_vector_type(16)));
typedef unsigned u32x2 __attribute__((ext_vector_type(2)));
typedef unsigned u32x4 __attribute__((ext_vector_type(4)));
#define LAS __attribute__((address_space(3)))
#define GAS __attribute__((address_space(1)))

constexpr int S = 2048, D = 512, F = 1376;
constexpr int NUNIT = F / 16;
constexpr int UR_PITCH = 2304;

constexpr size_t WS_UR = 0;
constexpr size_t WS_HF = WS_UR + (size_t)D * UR_PITCH * 2;
constexpr size_t WS_WF = WS_HF + (size_t)64 * NUNIT * 1024;
constexpr size_t WS_Y2 = WS_WF + (size_t)8 * NUNIT * 2048;
constexpr size_t WS_G  = WS_Y2 + (size_t)D * S * 2;
constexpr size_t WS_END = WS_G + S * 4;

__device__ __forceinline__ float bf_lo(unsigned x) { return __uint_as_float(x << 16); }
__device__ __forceinline__ float bf_hi(unsigned x) { return __uint_as_float(x & 0xffff0000u); }
__device__ __forceinline__ unsigned pk_bf16(float a, float b) {
    f32x2 t = {a, b}; bf16x2 p = __builtin_convertvector(t, bf16x2); return __builtin_bit_cast(unsigned, p);
}
__device__ __forceinline__ float rbf(float a) { return bf_lo(pk_bf16(a, a)); }

__device__ __forceinline__ void dma16(const void* sbase, unsigned voff, unsigned lds) {
    unsigned keep;
    asm volatile("s_mov_b32 %0, m0\n\ts_mov_b32 m0, %3\n\ts_nop 0\n\tglobal_load_lds_dwordx4 %1, %2\n\ts_mov_b32 m0, %0"
                 : "=&s"(keep) : "v"(voff), "s"(sbase), "s"(lds) : "memory");
}
#define WAIT_VM(n) asm volatile("s_waitcnt vmcnt(" #n ")" ::: "memory")
#define WAIT_LGKM0() asm volatile("s_waitcnt lgkmcnt(0)" ::: "memory")
#define BARRIER() do { asm volatile("s_waitcnt lgkmcnt(0)\n\ts_barrier" ::: "memory"); } while (0)

__device__ __forceinline__ s16x4 tr_read(unsigned addr) {
    return __builtin_amdgcn_ds_read_tr16_b64_v4i16((LAS s16x4*)addr);
}
__device__ __forceinline__ bf16x8 cat8(s16x4 a, s16x4 b) {
    return __builtin_bit_cast(bf16x8, __builtin_shufflevector(a, b, 0, 1, 2, 3, 4, 5, 6, 7));
}
__device__ __forceinline__ bf16x8 lds_read128(unsigned addr) { return *(LAS bf16x8*)addr; }

__device__ __forceinline__ float gelu_mul(float g, float u) {
    const float c1 = -2.0f * 0.7978845608028654f * 1.4426950408889634f;
    const float c2 = c1 * 0.044715f;
    float t = g * (c1 + c2 * g * g);
    float e = __builtin_amdgcn_exp2f(t);
    float r = __builtin_amdgcn_rcpf(1.0f + e);
    return g * u * r;
}

struct Params {
    const u16 *q, *k, *v, *shift_k, *ssm_k, *ssm_D, *wg, *wu, *wd, *gw;
    u16* out;
    char* ws;
};

constexpr int G1_STAGE = 40960;

__global__ __launch_bounds__(512, 2) void k1_kernel(Params p) {
    extern __shared__ __attribute__((aligned(16))) char smem[];
    const unsigned lds0 = (unsigned)(size_t)(LAS char*)smem;
    const int b = blockIdx.x, xcd = b & 7, r = b >> 3;
    const int tid = threadIdx.x, l = tid & 63;
    const int w = __builtin_amdgcn_readfirstlane(tid >> 6);
    const int rt = 4 * xcd + (r >> 3), c = r & 7;
    const int mt = 8 * (xcd >> 2) + (r >> 2), cj = xcd & 3, ci = r & 3;
    int st32 = cj < 3 ? 11 * cj + 3 * ci : (ci < 3 ? 33 + 3 * ci : 41);
    int wd32 = cj < 3 ? (ci < 3 ? 3 : 2) : (ci < 2 ? 3 : 2);
    int ld32 = st32 > 40 ? 40 : st32;
    const int own_lo = 2 * (st32 - ld32), own_hi = own_lo + 2 * wd32;
    const int col0 = ld32 * 32;

    const unsigned scr = lds0 + 2 * G1_STAGE;
    {
        int row = 64 * rt - 4 + 8 * w + (l >> 3);
        int rowc = row < 0 ? 0 : row;
        dma16(p.k, (unsigned)(rowc * 1024 + c * 128 + (l & 7) * 16), scr + w * 1024);
        if (w == 0) {
            int row2 = 64 * rt + 60 + (l >> 3); if (row2 > 64 * rt + 63) row2 = 64 * rt + 63;
            dma16(p.k, (unsigned)(row2 * 1024 + c * 128 + (l & 7) * 16), scr + 8 * 1024);
        }
        dma16(p.v, (unsigned)((64 * rt + 8 * w + (l >> 3)) * 1024 + c * 128 + (l & 7) * 16), scr + 9216 + w * 1024);
        if (w == 2 || (w == 3 && rt < 11)) {
            int ks = rt + 32 * (w - 2);
            for (int i = 0; i < 4; ++i)
                dma16(p.wd, (unsigned)((32 * ks + 8 * i + (l >> 3)) * 1024 + c * 128 + (l & 7) * 16), scr + 17408 + (w - 2) * 4096 + i * 1024);
        }
    }
    unsigned xoff[2], woff[3];
    {
        for (int i = 0; i < 2; ++i) {
            int row = 16 * w + 8 * i + (l >> 3);
            int ch = (l & 7) ^ ((row >> 1) & 7);
            xoff[i] = (unsigned)((128 * mt + row) * 1024 + ch * 16);
        }
        for (int i = 0; i < 3; ++i) {
            int n = 64 * i + l, row = n / 12, cc = n % 12;
            int src = (cc & ~3) | ((cc & 3) ^ (2 * ((row >> 3) & 1)));
            woff[i] = (unsigned)((16 * (w & 3) + row) * (F * 2) + col0 * 2 + src * 16);
        }
    }
    const u16* wsrc = (w >> 2) ? p.wu : p.wg;
    auto stage_issue = [&](int kt, int stg) {
        const char* xb = (const char*)p.q + kt * 128;
        const char* wb = (const char*)wsrc + (size_t)kt * 64 * (F * 2);
        unsigned sb = lds0 + stg * G1_STAGE;
        dma16(xb, xoff[0], sb + (2 * w) * 1024);
        dma16(xb, xoff[1], sb + (2 * w + 1) * 1024);
        unsigned wl = sb + 16384 + w * 3072;
        dma16(wb, woff[0], wl);
        dma16(wb, woff[1], wl + 1024);
        dma16(wb, woff[2], wl + 2048);
    };
    stage_issue(0, 0);
    stage_issue(1, 1);
    {
        int row = 128 * mt + 8 * (4 * cj + ci) + w;
        u32x4 xv = *(const u32x4*)((const char*)p.q + row * 1024 + l * 16);
        u32x4 gv = *(const u32x4*)((const char*)p.gw + l * 16);
        float acc = 0.f;
        for (int i = 0; i < 4; ++i) {
            unsigned pr = pk_bf16(bf_lo(xv[i]) * bf_lo(gv[i]), bf_hi(xv[i]) * bf_hi(gv[i]));
            acc += bf_lo(pr) + bf_hi(pr);
        }
        for (int m = 32; m >= 1; m >>= 1) acc += __shfl_xor(acc, m);
        float z = rbf(acc);
        float g = rbf(1.0f / (1.0f + __expf(-z)));
        if (l == 0) ((float*)(p.ws + WS_G))[row] = g;
    }
    WAIT_VM(0);
    BARRIER();
    {
        const int cg4 = w & 3, rh = w >> 2, q = l >> 4;
        const int r0 = 32 * rh + 8 * q;
        unsigned ka = scr + (r0 + ((l >> 2) & 3)) * 128 + cg4 * 32 + (l & 3) * 8;
        s16x4 k0 = tr_read(ka), k1 = tr_read(ka + 512), k2 = tr_read(ka + 1024);
        unsigned va = scr + 9216 + (r0 + ((l >> 2) & 3)) * 128 + cg4 * 32 + (l & 3) * 8;
        s16x4 v0 = tr_read(va), v1 = tr_read(va + 512);
        const int ch = 64 * c + 16 * cg4 + (l & 15);
        u32x2 sk = *(const u32x2*)(p.shift_k + ch * 4);
        float s0 = bf_lo(sk[0]), s1 = bf_hi(sk[0]), s2 = bf_lo(sk[1]), s3 = bf_hi(sk[1]);
        float kk[12], vv[8];
        for (int i = 0; i < 4; ++i) {
            kk[i] = __uint_as_float((unsigned)(u16)k0[i] << 16);
            kk[4 + i] = __uint_as_float((unsigned)(u16)k1[i] << 16);
            kk[8 + i] = __uint_as_float((unsigned)(u16)k2[i] << 16);
            vv[i] = __uint_as_float((unsigned)(u16)v0[i] << 16);
            vv[4 + i] = __uint_as_float((unsigned)(u16)v1[i] << 16);
        }
        if (rt == 0 && r0 == 0) { kk[0] = kk[1] = kk[2] = kk[3] = 0.f; }
        float kv[8];
        for (int i = 0; i < 8; ++i) {
            float a = rbf(kk[i + 1] * s3);
            a = rbf(a + rbf(kk[i + 2] * s2));
            a = rbf(a + rbf(kk[i + 3] * s1));
            a = rbf(a + rbf(kk[i + 4] * s0));
            kv[i] = a * vv[i];
        }
        u32x4 o = {pk_bf16(kv[7], kv[6]), pk_bf16(kv[5], kv[4]), pk_bf16(kv[3], kv[2]), pk_bf16(kv[1], kv[0])};
        int sg = 64 * rt + r0;
        *(u32x4*)(p.ws + WS_UR + (size_t)ch * (UR_PITCH * 2) + (2040 - sg) * 2) = o;
        if (rt == 0) {
            for (int i = 0; i < 4; ++i) {
                int n = i * 512 + tid;
                *(u32x4*)(p.ws + WS_UR + (size_t)(64 * c + (n >> 5)) * (UR_PITCH * 2) + 4096 + (n & 31) * 16) = (u32x4){0, 0, 0, 0};
            }
        }
        if (w == 2 || (w == 3 && rt < 11)) {
            int ks = rt + 32 * (w - 2);
            unsigned base = scr + 17408 + (w - 2) * 4096;
            const int g4 = l >> 4;
            for (int uu = 0; uu < 2; ++uu)
                for (int dh = 0; dh < 2; ++dh) {
                    int pp = l & 3;
                    int dcol = 32 * dh + 16 * (g4 & 1) + 8 * (pp & 1) + 4 * (pp >> 1);
                    unsigned a0 = base + (16 * uu + 8 * (g4 >> 1) + ((l >> 2) & 3)) * 128 + dcol * 2;
                    s16x4 t0 = tr_read(a0), t1 = tr_read(a0 + 512);
                    bf16x8 fr = cat8(t0, t1);
                    *(bf16x8*)(p.ws + WS_WF + ((size_t)(c * NUNIT + 2 * ks + uu) * 2 + dh) * 1024 + l * 16) = fr;
                }
        }
    }
    BARRIER();

    const int wm = w & 3, wn = w >> 2;
    unsigned xa[2], wa[3];
    {
        int fr = l & 15, q = l >> 4;
        for (int kk = 0; kk < 2; ++kk) xa[kk] = lds0 + (32 * wm + fr) * 128 + 16 * ((4 * kk + q) ^ (fr >> 1));
        int qq = (l >> 2) & 3, pp = l & 3;
        for (int j = 0; j < 3; ++j) {
            int u = 3 * wn + j;
            wa[j] = lds0 + 16384 + (8 * q + qq) * 192 + (u >> 1) * 64 + 16 * ((2 * (u & 1) + (pp >> 1)) ^ (2 * (q & 1))) + 8 * (pp & 1);
        }
    }
    f32x4 acc[2][3][2];
    for (int m = 0; m < 2; ++m) for (int j = 0; j < 3; ++j) for (int s = 0; s < 2; ++s) acc[m][j][s] = (f32x4){0, 0, 0, 0};
    int stg = 0;
    for (int kt = 0; kt < 8; ++kt) {
        if (kt >= 2) { if (kt < 7) WAIT_VM(5); else WAIT_VM(0); }
        if (kt > 0) BARRIER();
        if (kt + 2 < 8) { int s2 = stg + 2; if (s2 >= 3) s2 -= 3; stage_issue(kt + 2, s2); }
        const unsigned so = stg * G1_STAGE;
#pragma unroll
        for (int kk = 0; kk < 2; ++kk) {
            bf16x8 xb[2];
            for (int s = 0; s < 2; ++s) xb[s] = lds_read128(xa[kk] + so + s * 2048);
            for (int m = 0; m < 2; ++m)
                for (int j = 0; j < 3; ++j) {
                    unsigned a = wa[j] + so + m * 12288 + kk * 32 * 192;
                    bf16x8 af = cat8(tr_read(a), tr_read(a + 4 * 192));
                    for (int s = 0; s < 2; ++s) acc[m][j][s] = __builtin_amdgcn_mfma_f32_16x16x32_bf16(af, xb[s], acc[m][j][s], 0, 0, 0);
                }
        }
        stg = stg == 2 ? 0 : stg + 1;
    }
    for (int j = 0; j < 3; ++j) {
        unsigned P[2][2];
        for (int s = 0; s < 2; ++s) {
            float h0 = gelu_mul(acc[0][j][s][0], acc[1][j][s][0]);
            float h1 = gelu_mul(acc[0][j][s][1], acc[1][j][s][1]);
            float h2 = gelu_mul(acc[0][j][s][2], acc[1][j][s][2]);
            float h3 = gelu_mul(acc[0][j][s][3], acc[1][j][s][3]);
            P[s][0] = pk_bf16(h0, h1); P[s][1] = pk_bf16(h2, h3);
        }
        auto r0 = __builtin_amdgcn_permlane16_swap(P[0][0], P[1][0], false, false);
        auto r1 = __builtin_amdgcn_permlane16_swap(P[0][1], P[1][1], false, false);
        u32x4 o = {r0[0], r1[0], r0[1], r1[1]};
        int u = 3 * wn + j;
        if (u >= own_lo && u < own_hi)
            *(u32x4*)(p.ws + WS_HF + ((size_t)(4 * mt + wm) * NUNIT + 2 * ld32 + u) * 1024 + l * 16) = o;
    }
}

constexpr int HC_STRIDE = 4160, HC_BYTES = 4 * HC_STRIDE;
constexpr int UC_STRIDE = 4576, UC_BYTES = 2 * UC_STRIDE;
__global__ __launch_bounds__(512, 2) void k2_kernel(Params p) {
    extern __shared__ __attribute__((aligned(16))) char smem[];
    const unsigned lds0 = (unsigned)(size_t)(LAS char*)smem;
    const int b = blockIdx.x, xcd = b & 7, r = b >> 3;
    const int tid = threadIdx.x, l = tid & 63;
    const int w = __builtin_amdgcn_readfirstlane(tid >> 6);
    const int rt = 4 * xcd + (r >> 3), c = r & 7;
    const int ch0 = 64 * c + 2 * rt;
    const unsigned HC = lds0;
    const unsigned UC = lds0 + 2 * HC_BYTES;
    const unsigned RED = UC + 2 * UC_BYTES + 1024;
    {
        const int chn = w >> 2, pc = w & 3;
        const char* hb = (const char*)(p.ssm_k + (size_t)(ch0 + chn) * S);
        for (int i = 0; i < 4; ++i) {
            int chunk = 4 + 64 * i + l; if (chunk > 258) chunk = 258;
            int idx = 8 * chunk + 2 * pc - 30;
            dma16(hb, (unsigned)(idx * 2), HC + chn * HC_BYTES + pc * HC_STRIDE + 64 + i * 1024);
        }
        if (l < 4) {
            u32x4 o = {0, 0, 0, 0};
            if (l == 3) {
                u32x4 hv = *(const u32x4*)hb;
                if (pc == 3) o = hv;
                else if (pc == 2) o = (u32x4){0, hv[0], hv[1], hv[2]};
                else if (pc == 1) o = (u32x4){0, 0, hv[0], hv[1]};
                else o = (u32x4){0, 0, 0, hv[0]};
            }
            *(LAS u32x4*)(HC + chn * HC_BYTES + pc * HC_STRIDE + l * 16) = o;
        }
        const int pu = (w >> 1) & 1, hf = w & 1;
        const char* ub = p.ws + WS_UR + (size_t)(ch0 + chn) * (UR_PITCH * 2) + (pu ? 0 : 2);
        if (hf == 0) {
            for (int i = 0; i < 3; ++i) dma16(ub, (unsigned)((64 * i + l) * 16), UC + chn * UC_BYTES + pu * UC_STRIDE + i * 1024);
        } else {
            dma16(ub, (unsigned)((192 + l) * 16), UC + chn * UC_BYTES + pu * UC_STRIDE + 3072);
            if (l < 28) dma16(ub, (unsigned)((256 + l) * 16), UC + chn * UC_BYTES + pu * UC_STRIDE + 4096);
        }
    }
    WAIT_VM(0);
    BARRIER();
    f32x4 acc[8];
    const int chn = (w >> 1) & 1, chalf = w & 1;
    if (w < 4) {
        const int i16 = l & 15, g = l >> 4;
        const unsigned ha = HC + chn * HC_BYTES + (i16 & 3) * HC_STRIDE + ((i16 >> 2) + g) * 16;
        const unsigned ua = UC + chn * UC_BYTES + (i16 & 1) * UC_STRIDE + (252 - 4 * (i16 >> 1) + g) * 16;
        for (int n = 0; n < 8; ++n) acc[n] = (f32x4){0, 0, 0, 0};
        for (int cc = 0; cc < 4; ++cc) {
            const int cphase = 4 * chalf + cc;
            bf16x8 uf[8];
#pragma unroll
            for (int e = 0; e < 8; ++e) uf[e] = lds_read128(ua + cphase * 64 - e * 512);
#pragma unroll
            for (int a = 0; a < 8; ++a) {
                bf16x8 hf = lds_read128(ha + cphase * 64 + a * 512);
#pragma unroll
                for (int e = 7 - a; e >= 0; --e) acc[a + e] = __builtin_amdgcn_mfma_f32_16x16x32_bf16(uf[e], hf, acc[a + e], 0, 0, 0);
            }
        }
        if (chalf) {
#pragma unroll
            for (int i = 0; i < 4; ++i) { *(LAS f32x4*)(RED + w * 4096 + i * 1024 + l * 16) = acc[i]; acc[i] = acc[4 + i]; }
        } else {
#pragma unroll
            for (int i = 0; i < 4; ++i) *(LAS f32x4*)(RED + w * 4096 + i * 1024 + l * 16) = acc[4 + i];
        }
    }
    BARRIER();
    if (w < 4) {
        const int j = l & 15, q = l >> 4;
        float Dd = __uint_as_float((unsigned)p.ssm_D[ch0 + chn] << 16);
        unsigned pk[8];
#pragma unroll
        for (int i = 0; i < 4; ++i) {
            f32x4 o = *(LAS f32x4*)(RED + (w ^ 1) * 4096 + i * 1024 + l * 16);
            o += acc[i];
            const int n = 4 * chalf + i;
#pragma unroll
            for (int hh = 0; hh < 2; ++hh) {
                int s = 256 * n + 2 * j + 32 * (2 * q + hh);
                unsigned uu = *(LAS unsigned*)(UC + chn * UC_BYTES + UC_STRIDE + (2046 - s) * 2);
                unsigned yy = pk_bf16(o[2 * hh], o[2 * hh + 1]);
                unsigned dd = pk_bf16(bf_hi(uu) * Dd, bf_lo(uu) * Dd);
                pk[2 * i + hh] = pk_bf16(bf_lo(yy) + bf_lo(dd), bf_hi(yy) + bf_hi(dd));
            }
        }
        WAIT_LGKM0();
        const unsigned yb = RED + (w ^ 1) * 4096;
#pragma unroll
        for (int i = 0; i < 4; ++i)
#pragma unroll
            for (int hh = 0; hh < 2; ++hh) *(LAS unsigned*)(yb + (256 * i + 2 * j + 32 * (2 * q + hh)) * 2) = pk[2 * i + hh];
        WAIT_LGKM0();
        for (int i = 0; i < 2; ++i) {
            u32x4 v = *(LAS u32x4*)(yb + i * 1024 + l * 16);
            *(u32x4*)(p.ws + WS_Y2 + (size_t)(ch0 + chn) * (S * 2) + chalf * 2048 + i * 1024 + l * 16) = v;
        }
    }
}

constexpr int G2_STAGE = 32768;
__global__ __launch_bounds__(512, 2) void k3_kernel(Params p) {
    extern __shared__ __attribute__((aligned(16))) char smem[];
    const unsigned lds0 = (unsigned)(size_t)(LAS char*)smem;
    const int b = blockIdx.x, xcd = b & 7, r = b >> 3;
    const int tid = threadIdx.x, l = tid & 63;
    const int w = __builtin_amdgcn_readfirstlane(tid >> 6);
    const int rt = 4 * xcd + (r >> 3), c = r & 7;
    const int kq = w & 3, dh = w >> 2;
    const unsigned Y2L = lds0 + 4 * G2_STAGE;
    const char* hbase = p.ws + WS_HF + (size_t)(2 * rt) * NUNIT * 1024;
    auto h_issue = [&](int t) {
        for (int i = 0; i < 4; ++i) {
            int bi = 4 * w + i, u = 16 * t + (bi >> 1), rt2 = bi & 1;
            if (u > NUNIT - 1) u = NUNIT - 1;
            dma16(hbase + (size_t)(rt2 * NUNIT + u) * 1024, (unsigned)(l * 16), lds0 + (t & 3) * G2_STAGE + bi * 1024);
        }
    };
    dma16(p.ws + WS_Y2 + (size_t)(64 * c + 8 * w) * (S * 2) + rt * 128, (unsigned)((l >> 3) * (S * 2) + (l & 7) * 16), Y2L + w * 1024);
    h_issue(0); h_issue(1); h_issue(2);
    bf16x8 wf[22];
    {
        const char* wb = p.ws + WS_WF + (size_t)(c * NUNIT) * 2048 + dh * 1024 + l * 16;
#pragma unroll
        for (int i = 0; i < 22; ++i) { int u = 4 * i + kq; if (u > NUNIT - 1) u = NUNIT - 1; wf[i] = *(const bf16x8*)(wb + (size_t)u * 2048); }
    }
    f32x16 acc[2];
    for (int i = 0; i < 16; ++i) { acc[0][i] = 0.f; acc[1][i] = 0.f; }
    WAIT_VM(0);
#pragma unroll
    for (int t = 0; t < 6; ++t) {
        if (t >= 3) { if (t < 5) WAIT_VM(8); else WAIT_VM(0); }
        if (t == 3) WAIT_VM(8);
        if (t == 4) WAIT_VM(4);
        BARRIER();
        if (t + 3 < 6) h_issue(t + 3);
#pragma unroll
        for (int i = 0; i < 4; ++i) {
            const int idx = 4 * t + i;
            if (idx < 21 || (idx == 21 && kq < 2)) {
                if (idx < 22) {
                    unsigned a = lds0 + (t & 3) * G2_STAGE + (4 * i + kq) * 2048 + l * 16;
                    bf16x8 h0 = lds_read128(a), h1 = lds_read128(a + 1024);
                    acc[0] = __builtin_amdgcn_mfma_f32_32x32x16_bf16(wf[idx < 22 ? idx : 0], h0, acc[0], 0, 0, 0);
                    acc[1] = __builtin_amdgcn_mfma_f32_32x32x16_bf16(wf[idx < 22 ? idx : 0], h1, acc[1], 0, 0, 0);
                }
            }
        }
    }
    BARRIER();
#pragma unroll
    for (int qd = 0; qd < 4; ++qd) {
        if (qd != kq) {
            unsigned a = lds0 + ((dh * 4 + qd) * 4 + kq) * 2048 + l * 16;
            f32x16 v = acc[qd >> 1];
            int o = 8 * (qd & 1);
            *(LAS f32x4*)(a) = (f32x4){v[o], v[o + 1], v[o + 2], v[o + 3]};
            *(LAS f32x4*)(a + 1024) = (f32x4){v[o + 4], v[o + 5], v[o + 6], v[o + 7]};
        }
    }
    float bsum[8];
    {
        f32x16 v = (kq >> 1) ? acc[1] : acc[0];
        for (int i = 0; i < 8; ++i) bsum[i] = (kq & 1) ? v[8 + i] : v[i];
    }
    BARRIER();
#pragma unroll
    for (int src = 0; src < 4; ++src) {
        if (src != kq) {
            unsigned a = lds0 + ((dh * 4 + kq) * 4 + src) * 2048 + l * 16;
            f32x4 v0 = *(LAS f32x4*)(a), v1 = *(LAS f32x4*)(a + 1024);
            for (int i = 0; i < 4; ++i) { bsum[i] += v0[i]; bsum[4 + i] += v1[i]; }
        }
    }
    const int sl = 32 * (kq >> 1) + (l & 31), dl = 32 * dh + 16 * (kq & 1) + 8 * (l >> 5);
    const int sg = 64 * rt + sl, dg = 64 * c + dl;
    u32x4 qv = *(const u32x4*)(p.q + (size_t)sg * D + dg);
    float g = ((const float*)(p.ws + WS_G))[sg];
    unsigned ya = Y2L + (dl + ((l >> 2) & 3)) * 128 + (32 * (kq >> 1) + 16 * ((l >> 4) & 1) + 4 * (l & 3)) * 2;
    s16x4 y0 = tr_read(ya), y1 = tr_read(ya + 512);
    float omg = rbf(1.0f - g);
    unsigned o[4];
    for (int i = 0; i < 4; ++i) {
        float ya0 = __uint_as_float((unsigned)(u16)(i < 2 ? y0[2 * i] : y1[2 * i - 4]) << 16);
        float ya1 = __uint_as_float((unsigned)(u16)(i < 2 ? y0[2 * i + 1] : y1[2 * i - 3]) << 16);
        unsigned av = pk_bf16(bf_lo(qv[i]) * ya0, bf_hi(qv[i]) * ya1);
        unsigned bv = pk_bf16(bsum[2 * i], bsum[2 * i + 1]);
        unsigned t1 = pk_bf16(g * bf_lo(av), g * bf_hi(av));
        unsigned t2 = pk_bf16(omg * bf_lo(bv), omg * bf_hi(bv));
        o[i] = pk_bf16(bf_lo(t1) + bf_lo(t2), bf_hi(t1) + bf_hi(t2));
    }
    *(u32x4*)(p.out + (size_t)sg * D + dg) = (u32x4){o[0], o[1], o[2], o[3]};
}

extern "C" void kernel_launch(void* const* d_in, const int* in_sizes, int n_in,
                              void* d_out, int out_size, void* d_ws, size_t ws_size,
                              hipStream_t stream) {
    Params p;
    p.q = (const u16*)d_in[0]; p.k = (const u16*)d_in[1]; p.v = (const u16*)d_in[2];
    p.shift_k = (const u16*)d_in[3]; p.ssm_k = (const u16*)d_in[4]; p.ssm_D = (const u16*)d_in[5];
    p.wg = (const u16*)d_in[7]; p.wu = (const u16*)d_in[8]; p.wd = (const u16*)d_in[9]; p.gw = (const u16*)d_in[10];
    p.out = (u16*)d_out; p.ws = (char*)d_ws;
    const int l1 = 3 * G1_STAGE, l2 = 2 * HC_BYTES + 2 * UC_BYTES + 1024 + 16384, l3 = 4 * G2_STAGE + 8192;
    (void)hipFuncSetAttribute((const void*)k1_kernel, hipFuncAttributeMaxDynamicSharedMemorySize, l1);
    (void)hipFuncSetAttribute((const void*)k2_kernel, hipFuncAttributeMaxDynamicSharedMemorySize, l2);
    (void)hipFuncSetAttribute((const void*)k3_kernel, hipFuncAttributeMaxDynamicSharedMemorySize, l3);
    k1_kernel<<<256, 512, l1, stream>>>(p);
    k2_kernel<<<256, 512, l2, stream>>>(p);
    k3_kernel<<<256, 512, l3, stream>>>(p);
}
```

```cpp
#include <hip/hip_runtime.h>
#include <hip/hip_bf16.h>
#include <stdint.h>

typedef unsigned short u16;
typedef unsigned long long u64;
typedef short s16x4 __attribute__((ext_vector_type(4)));
typedef __bf16 bf16x8 __attribute__((ext_vector_type(8)));
typedef __bf16 bf16x2 __attribute__((ext_vector_type(2)));
typedef float f32x2 __attribute__((ext_vector_type(2)));
typedef float f32x4 __attribute__((ext_vector_type(4)));
typedef float f32x16 __attribute__((ext_vector_type(16)));
typedef unsigned u32x2 __attribute__((ext_vector_type(2)));
typedef unsigned u32x4 __attribute__((ext_vector_type(4)));
#define LAS __attribute__((address_space(3)))
#define GAS __attribute__((address_space(1)))

constexpr int S = 2048, D = 512, F = 1376;
constexpr int NUNIT = F / 16;
constexpr int UR_PITCH = 2304;

constexpr unsigned WS_EP = 0;
constexpr unsigned WS_F1 = 4096;
constexpr unsigned WS_F2 = WS_F1 + 2048;
constexpr unsigned WS_F3 = WS_F2 + 8192;
constexpr unsigned WS_G  = 32768;
constexpr unsigned WS_UR = 65536;
constexpr unsigned WS_HF = WS_UR + D * UR_PITCH * 2;
constexpr unsigned WS_WF = WS_HF + 64 * NUNIT * 1024;
constexpr unsigned WS_Y2 = WS_WF + 8 * NUNIT * 2048;
constexpr unsigned WS_END = WS_Y2 + D * S * 2;

constexpr unsigned G1_STAGE = 41024, G1_WG = 16384, G1_WU = 16384 + 12288 + 32;
constexpr unsigned L_HC = 3 * G1_STAGE;
constexpr unsigned HC_STRIDE = 4160, HC_BYTES = 4 * HC_STRIDE;
constexpr unsigned L_UC = 2 * G1_STAGE;
constexpr unsigned UC_STRIDE = 4576, UC_BYTES = 2 * UC_STRIDE;
constexpr unsigned L_RED = L_UC + 2 * UC_BYTES + 128;
constexpr unsigned L_SCR = L_HC;
constexpr unsigned SC_K = 0, SC_V = 9216, SC_WD = 17408, SC_X = 25600, SC_GW = 33792, SC_SK = 34816;
constexpr unsigned L_FLAG = 163840 - 64;
constexpr unsigned L_Y2 = 155648;
constexpr unsigned L_P = 32768;
constexpr unsigned L_GA = 65536;
constexpr unsigned LDS_BYTES = 163840;

__device__ __forceinline__ float bf_lo(unsigned x) { return __uint_as_float(x << 16); }
__device__ __forceinline__ float bf_hi(unsigned x) { return __uint_as_float(x & 0xffff0000u); }
__device__ __forceinline__ unsigned pk_bf16(float a, float b) {
    f32x2 t = {a, b}; bf16x2 p = __builtin_convertvector(t, bf16x2); return __builtin_bit_cast(unsigned, p);
}
__device__ __forceinline__ float rbf(float a) { return bf_lo(pk_bf16(a, a)); }

__device__ __forceinline__ void dma16(const void* sbase, unsigned voff, unsigned lds) {
    asm volatile("s_mov_b32 m0, %2\n\ts_nop 0\n\tglobal_load_lds_dwordx4 %0, %1" :: "v"(voff), "s"(sbase), "s"(lds) : "memory");
}
__device__ __forceinline__ void dma16c(const void* sbase, unsigned voff, unsigned lds) {
    asm volatile("s_mov_b32 m0, %2\n\ts_nop 0\n\tglobal_load_lds_dwordx4 %0, %1 sc1" :: "v"(voff), "s"(sbase), "s"(lds) : "memory");
}
__device__ __forceinline__ void dma4(const void* sbase, unsigned voff, unsigned lds) {
    asm volatile("s_mov_b32 m0, %2\n\ts_nop 0\n\tglobal_load_lds_dword %0, %1" :: "v"(voff), "s"(sbase), "s"(lds) : "memory");
}
#define WAIT_VM(n) asm volatile("s_waitcnt vmcnt(" #n ")" ::: "memory")
#define WAIT_LGKM0() asm volatile("s_waitcnt lgkmcnt(0)" ::: "memory")
#define BARRIER() asm volatile("s_waitcnt lgkmcnt(0)\n\ts_barrier" ::: "memory")
#define SB() __builtin_amdgcn_sched_barrier(0)

__device__ __forceinline__ s16x4 tr_read(unsigned addr) { return __builtin_amdgcn_ds_read_tr16_b64_v4i16((LAS s16x4*)addr); }
__device__ __forceinline__ bf16x8 cat8(s16x4 a, s16x4 b) {
    return __builtin_bit_cast(bf16x8, __builtin_shufflevector(a, b, 0, 1, 2, 3, 4, 5, 6, 7));
}
__device__ __forceinline__ bf16x8 lds_read128(unsigned addr) { return *(LAS bf16x8*)addr; }
__device__ __forceinline__ float s2f(short x) { return __uint_as_float((unsigned)(u16)x << 16); }

__device__ __forceinline__ float gelu_mul(float g, float u) {
    const float c1 = -2.0f * 0.7978845608028654f * 1.4426950408889634f;
    const float c2 = c1 * 0.044715f;
    float t = g * (c1 + c2 * g * g);
    float e = __builtin_amdgcn_exp2f(t);
    float r = __builtin_amdgcn_rcpf(1.0f + e);
    return g * u * r;
}

#define STAMP_DECL
#define STAMP(i)
#define STAMP_FLUSH(kid)

struct Params {
    const u16 *q, *k, *v, *shift_k, *ssm_k, *ssm_D, *wg, *wu, *wd, *gw;
    u16* out;
    char* ws;
};

#define HFLAG_AT 0
#define SPIN_CAP (1 << 18)
__device__ __forceinline__ void poll_flags(const char* ws, unsigned voff, u64 tag) {
#pragma unroll 1
    for (int it = 0; it < SPIN_CAP; ++it) {
        u64 v;
        asm volatile("global_load_dwordx2 %0, %1, %2 sc1\n\ts_waitcnt vmcnt(0)" : "=&v"(v) : "v"(voff), "s"(ws) : "memory");
        if (__all(v == tag)) break;
        __builtin_amdgcn_s_sleep(1);
    }
}
__device__ __forceinline__ void flag_store(char* ws, unsigned off, u64 tag) {
    __hip_atomic_store((GAS u64*)(ws + off), tag, __ATOMIC_RELAXED, __HIP_MEMORY_SCOPE_AGENT);
}

__global__ __launch_bounds__(512, 2) void fused_kernel(Params p) {
    extern __shared__ __attribute__((aligned(16))) char smem[];
    const unsigned lds0 = (unsigned)(size_t)(LAS char*)smem;
    const int b = blockIdx.x, xcd = b & 7, r = b >> 3;
    const int tid = threadIdx.x, l = tid & 63;
    const int w = __builtin_amdgcn_readfirstlane(tid >> 6);
    const int rt = 4 * xcd + (r >> 3), c = r & 7;
    const int mt = 8 * (xcd >> 2) + (r >> 2), cj = xcd & 3, ci = r & 3, ct = 4 * cj + ci;
    const int st32 = cj < 3 ? 11 * cj + 3 * ci : (ci < 3 ? 33 + 3 * ci : 41);
    const int wd32 = cj < 3 ? (ci < 3 ? 3 : 2) : (ci < 2 ? 3 : 2);
    const int ld32 = st32 > 40 ? 40 : st32;
    const int own_lo = 2 * (st32 - ld32), own_hi = own_lo + 2 * wd32;
    const int ch0 = 64 * c + 2 * rt;
    u64 tag = *(const u64*)(p.ws + WS_EP) + 1;
    unsigned dpair = *(const unsigned*)(p.ssm_D + ch0);
    STAMP_DECL STAMP(0);
    const __amdgpu_buffer_rsrc_t wsr = __builtin_amdgcn_make_buffer_rsrc(p.ws, 0, WS_END, 0x00020000);
    const unsigned scr = lds0 + L_SCR;
    const int grow = 128 * mt + 8 * ct + w;

    {
        const unsigned colb = (unsigned)(c * 128 + (l & 7) * 16);
        int row = 64 * rt - 4 + 8 * w + (l >> 3);
        int rowc = row < 0 ? 0 : row;
        dma16(p.k, (unsigned)(rowc * 1024) + colb, scr + SC_K + w * 1024);
        dma16(p.v, (unsigned)((64 * rt + 8 * w + (l >> 3)) * 1024) + colb, scr + SC_V + w * 1024);
        dma16(p.q, (unsigned)(grow * 1024 + l * 16), scr + SC_X + w * 1024);
        if (w == 0) {
            int row2 = 64 * rt + 60 + ((l >> 3) & 3);
            dma16(p.k, (unsigned)(row2 * 1024) + colb, scr + SC_K + 8 * 1024);
            if (l < 16) *(LAS unsigned*)(lds0 + L_FLAG + l * 4) = 0;
        }
        if (w == 1) dma16(p.gw, (unsigned)(l * 16), scr + SC_GW);
        if (w == 2) { if (l < 32) dma16(p.shift_k, (unsigned)(c * 512 + l * 16), scr + SC_SK); }
        if (w < 2 || (w < 4 && rt < 11)) {
            const int un = 2 * (rt + 32 * (w >> 1)) + (w & 1);
            for (int i = 0; i < 2; ++i)
                dma16(p.wd, (unsigned)((16 * un + 8 * i + (l >> 3)) * 1024) + colb, scr + SC_WD + w * 2048 + i * 1024);
        }
    }
    const int xw = w & 3;
    unsigned xoff[2], woff[3];
    {
        for (int i = 0; i < 2; ++i) {
            int row = 8 * i + (l >> 3);
            xoff[i] = (unsigned)(row * 1024 + (((l & 7) ^ ((row >> 1) & 7)) * 16));
        }
        for (int i = 0; i < 3; ++i) {
            int n = 64 * i + l, row = n / 12, cc = n % 12;
            woff[i] = (unsigned)(row * (F * 2) + cc * 16);
        }
    }
    const char* xsrc = (const char*)p.q + (size_t)(128 * mt + 32 * xw) * 1024;
    const char* wsrc = (const char*)((xw >> 1) ? p.wu : p.wg) + (size_t)(32 * (xw & 1)) * (F * 2) + ld32 * 64;
    auto tile_issue = [&](int kt) {
        const unsigned sb = lds0 + (kt % 3) * G1_STAGE;
        const char* xb = xsrc + kt * 128;
        const char* wb = wsrc + (size_t)kt * 64 * (F * 2);
#pragma unroll
        for (int i = 0; i < 4; ++i) dma16(xb + (i >> 1) * 16384, xoff[i & 1], sb + (4 * xw + i) * 1024);
        const unsigned wl = sb + ((xw >> 1) ? G1_WU : G1_WG) + (xw & 1) * 6144;
#pragma unroll
        for (int i = 0; i < 6; ++i) dma16(wb + (i / 3) * 16 * (F * 2), woff[i % 3], wl + i * 1024);
    };
    if (w >= 4) { tile_issue(0); tile_issue(1); tile_issue(2); WAIT_VM(30); } else { WAIT_VM(0); }
    asm volatile("" : "+s"(tag), "+s"(dpair));
    BARRIER();
    STAMP(1);
    {
        const int cg4 = w & 3, rh = w >> 2, q = l >> 4;
        const int r0 = 32 * rh + 8 * q;
        unsigned ka = scr + SC_K + (r0 + ((l >> 2) & 3)) * 128 + cg4 * 32 + (l & 3) * 8;
        s16x4 k0 = tr_read(ka), k1 = tr_read(ka + 512), k2 = tr_read(ka + 1024);
        s16x4 v0 = tr_read(ka + SC_V), v1 = tr_read(ka + SC_V + 512);
        u32x2 sk = *(LAS u32x2*)(scr + SC_SK + (16 * cg4 + (l & 15)) * 8);
        u32x4 xv = *(LAS u32x4*)(scr + SC_X + w * 1024 + l * 16);
        u32x4 gv = *(LAS u32x4*)(scr + SC_GW + l * 16);
        const int ch = 64 * c + 16 * cg4 + (l & 15);
        float s0 = bf_lo(sk[0]), s1 = bf_hi(sk[0]), s2 = bf_lo(sk[1]), s3 = bf_hi(sk[1]);
        float kk[12], vv[8];
        for (int i = 0; i < 4; ++i) {
            kk[i] = s2f(k0[i]); kk[4 + i] = s2f(k1[i]); kk[8 + i] = s2f(k2[i]);
            vv[i] = s2f(v0[i]); vv[4 + i] = s2f(v1[i]);
        }
        if (rt == 0 && r0 == 0) { kk[1] = kk[2] = kk[3] = 0.f; }
        float kv[8];
        for (int i = 0; i < 8; ++i) kv[i] = (kk[i + 1] * s3 + kk[i + 2] * s2 + kk[i + 3] * s1 + kk[i + 4] * s0) * vv[i];
        u32x4 o = {pk_bf16(kv[7], kv[6]), pk_bf16(kv[5], kv[4]), pk_bf16(kv[3], kv[2]), pk_bf16(kv[1], kv[0])};
        __builtin_amdgcn_raw_buffer_store_b128(o, wsr, WS_UR + ch * (UR_PITCH * 2) + (2040 - 64 * rt - r0) * 2, 0, 16);
        float acc = 0.f;
        for (int i = 0; i < 4; ++i) {
            unsigned pr = pk_bf16(bf_lo(xv[i]) * bf_lo(gv[i]), bf_hi(xv[i]) * bf_hi(gv[i]));
            acc += bf_lo(pr) + bf_hi(pr);
        }
        for (int m = 32; m >= 1; m >>= 1) acc += __shfl_xor(acc, m);
        float z = rbf(acc);
        float g = rbf(__builtin_amdgcn_rcpf(1.0f + __builtin_amdgcn_exp2f(-1.4426950408889634f * z)));
        if (l == 0) __builtin_amdgcn_raw_buffer_store_b32(__float_as_uint(g), wsr, WS_G + grow * 4, 0, 16);
        if (rt == 0) {
            for (int i = 0; i < 4; ++i) {
                int n = i * 512 + tid;
                __builtin_amdgcn_raw_buffer_store_b128((u32x4){0, 0, 0, 0}, wsr, WS_UR + (64 * c + (n >> 5)) * (UR_PITCH * 2) + 4096 + (n & 31) * 16, 0, 16);
            }
        }
        if (w < 2 || (w < 4 && rt < 11)) {
            const int un = 2 * (rt + 32 * (w >> 1)) + (w & 1);
            const int g4 = l >> 4, pp = l & 3;
            for (int dh = 0; dh < 2; ++dh) {
                int dcol = 32 * dh + 16 * (g4 & 1) + 8 * (pp & 1) + 4 * (pp >> 1);
                unsigned a0 = scr + SC_WD + w * 2048 + (8 * (g4 >> 1) + ((l >> 2) & 3)) * 128 + dcol * 2;
                bf16x8 fr = cat8(tr_read(a0), tr_read(a0 + 512));
                __builtin_amdgcn_raw_buffer_store_b128(__builtin_bit_cast(u32x4, fr), wsr, WS_WF + ((c * NUNIT + un) * 2 + dh) * 1024 + l * 16, 0, 16);
            }
        }
    }
    STAMP(2);

    if (w < 4) {
        const int wm = w & 1, wn = w >> 1;
        unsigned xa[4], wa;
        {
            const int sr = l & 31, hi = l >> 5;
            for (int ks = 0; ks < 4; ++ks) xa[ks] = lds0 + (64 * wm + sr) * 128 + 16 * ((2 * ks + hi) ^ ((sr >> 1) & 7));
            const int g4 = l >> 4, qq = (l >> 2) & 3, pp = l & 3;
            wa = lds0 + ((pp >> 1) ? G1_WU : G1_WG) + (8 * (g4 >> 1) + qq) * 192 + (3 * wn) * 32 + (2 * (pp & 1) + (g4 & 1)) * 8;
        }
        f32x16 acc[2][3];
        for (int s = 0; s < 2; ++s) for (int j = 0; j < 3; ++j) for (int i = 0; i < 16; ++i) acc[s][j][i] = 0.f;
        bf16x8 xb[2][2], af[2][3];
        BARRIER();
        STAMP(3);
        {
            xb[0][0] = lds_read128(xa[0]); xb[0][1] = lds_read128(xa[0] + 4096);
            for (int j = 0; j < 3; ++j) af[0][j] = cat8(tr_read(wa + j * 32), tr_read(wa + j * 32 + 768));
        }
        SB();
#pragma unroll
        for (int kt = 0; kt < 8; ++kt) {
#pragma unroll
            for (int ks = 0; ks < 4; ++ks) {
                const int cur = ks & 1, nx = cur ^ 1;
                const int nkt = ks == 3 ? kt + 1 : kt, nks = (ks + 1) & 3;
                const bool rd = nkt < 8;
                const unsigned so = (nkt % 3) * G1_STAGE;
                if (ks == 3 && kt < 7) {
                    if (kt == 1) WAIT_VM(0);
                    BARRIER();
                    SB();
                }
#pragma unroll
                for (int i = 0; i < 6; ++i) {
                    const int j = i >> 1, s = i & 1;
                    acc[s][j] = __builtin_amdgcn_mfma_f32_32x32x16_bf16(af[cur][j], xb[cur][s], acc[s][j], 0, 0, 0);
                    SB();
                    if (rd) {
                        if (i == 0) { xb[nx][0] = lds_read128(xa[nks] + so); unsigned a = wa + so + nks * 3072; af[nx][0] = cat8(tr_read(a), tr_read(a + 768)); }
                        if (i == 1) { xb[nx][1] = lds_read128(xa[nks] + so + 4096); }
                        if (i == 1 || i == 2) { unsigned a = wa + so + nks * 3072 + i * 32; af[nx][i] = cat8(tr_read(a), tr_read(a + 768)); }
                    }
                    SB();
                }
            }
        }
        STAMP(4);
#pragma unroll
        for (int j = 0; j < 3; ++j) {
            const int u = 3 * wn + j;
#pragma unroll
            for (int s = 0; s < 2; ++s) {
                float h[8];
#pragma unroll
                for (int i = 0; i < 4; ++i) {
                    h[i] = gelu_mul(acc[s][j][i], acc[s][j][4 + i]);
                    h[4 + i] = gelu_mul(acc[s][j][8 + i], acc[s][j][12 + i]);
                }
                u32x4 o = {pk_bf16(h[0], h[1]), pk_bf16(h[2], h[3]), pk_bf16(h[4], h[5]), pk_bf16(h[6], h[7])};
                if (u >= own_lo && u < own_hi)
                    __builtin_amdgcn_raw_buffer_store_b128(o, wsr, WS_HF + ((4 * mt + 2 * wm + s) * NUNIT + 2 * ld32 + u) * 1024 + l * 16, 0, 16);
            }
        }
        STAMP(5);
        BARRIER();
        STAMP(6);
        const int chn = w >> 1, chalf = w & 1;
        f32x4 ca[8];
        {
            const int i16 = l & 15, g = l >> 4;
            const unsigned ha = lds0 + L_HC + chn * HC_BYTES + (i16 & 3) * HC_STRIDE + ((i16 >> 2) + g) * 16 + chalf * 256;
            const unsigned ua = lds0 + L_UC + chn * UC_BYTES + (i16 & 1) * UC_STRIDE + (252 - 224 - 4 * (i16 >> 1) + g) * 16 + chalf * 256;
            for (int n = 0; n < 8; ++n) ca[n] = (f32x4){0, 0, 0, 0};
            bf16x8 uf[2][8], hf[2][8];
#pragma unroll
            for (int e = 7; e >= 0; --e) uf[0][e] = lds_read128(ua + (7 - e) * 512);
#pragma unroll
            for (int a = 0; a < 8; ++a) hf[0][a] = lds_read128(ha + a * 512);
            SB();
#pragma unroll
            for (int cc = 0; cc < 4; ++cc) {
                const int cur = cc & 1, nx = cur ^ 1;
                int cnt = 0;
#pragma unroll
                for (int a = 0; a < 8; ++a) {
#pragma unroll
                    for (int e = 7 - a; e >= 0; --e) {
                        ca[a + e] = __builtin_amdgcn_mfma_f32_16x16x32_bf16(uf[cur][e], hf[cur][a], ca[a + e], 0, 0, 0);
                        SB();
                        if (cc < 3 && (cnt & 1) == 0 && cnt < 32) {
                            const int ri = cnt >> 1;
                            if (ri == 0) hf[nx][0] = lds_read128(ha + (cc + 1) * 64);
                            else if (ri <= 8) uf[nx][8 - ri] = lds_read128(ua + (cc + 1) * 64 + (ri - 1) * 512);
                            else hf[nx][ri - 8] = lds_read128(ha + (cc + 1) * 64 + (ri - 8) * 512);
                            SB();
                        }
                        ++cnt;
                    }
                }
                if (cc == HFLAG_AT) {
                    WAIT_VM(0);
                    if (l == 0) flag_store(p.ws, WS_F2 + ((2 * mt + wm) * 32 + wn * 16 + ct) * 8, tag);
                    STAMP(7);
                    SB();
                }
            }
        }
        STAMP(8);
        {
            const unsigned red = lds0 + L_RED;
            if (chalf) {
#pragma unroll
                for (int i = 0; i < 4; ++i) { *(LAS f32x4*)(red + w * 4096 + i * 1024 + l * 16) = ca[i]; ca[i] = ca[4 + i]; }
            } else {
#pragma unroll
                for (int i = 0; i < 4; ++i) *(LAS f32x4*)(red + w * 4096 + i * 1024 + l * 16) = ca[4 + i];
            }
            WAIT_LGKM0();
            *(volatile LAS unsigned*)(lds0 + L_FLAG + w * 4) = 1;
            const int j = l & 15, q = l >> 4;
            const float Dd = chn ? bf_hi(dpair) : bf_lo(dpair);
            unsigned uu[8];
#pragma unroll
            for (int i = 0; i < 4; ++i)
#pragma unroll
                for (int hh = 0; hh < 2; ++hh) {
                    int s = 256 * (4 * chalf + i) + 2 * j + 32 * (2 * q + hh);
                    uu[2 * i + hh] = *(LAS unsigned*)(lds0 + L_UC + chn * UC_BYTES + UC_STRIDE + (2046 - s) * 2);
                }
#pragma unroll 1
            for (int it = 0; it < SPIN_CAP; ++it) { if (*(volatile LAS unsigned*)(lds0 + L_FLAG + (w ^ 1) * 4)) break; }
            asm volatile("" ::: "memory");
            unsigned pk[8];
#pragma unroll
            for (int i = 0; i < 4; ++i) {
                f32x4 o = *(LAS f32x4*)(red + (w ^ 1) * 4096 + i * 1024 + l * 16);
                o += ca[i];
#pragma unroll
                for (int hh = 0; hh < 2; ++hh) {
                    unsigned yy = pk_bf16(o[2 * hh], o[2 * hh + 1]);
                    unsigned dd = pk_bf16(bf_hi(uu[2 * i + hh]) * Dd, bf_lo(uu[2 * i + hh]) * Dd);
                    pk[2 * i + hh] = pk_bf16(bf_lo(yy) + bf_lo(dd), bf_hi(yy) + bf_hi(dd));
                }
            }
            WAIT_LGKM0();
            const unsigned yb = red + (w ^ 1) * 4096;
#pragma unroll
            for (int i = 0; i < 4; ++i)
#pragma unroll
                for (int hh = 0; hh < 2; ++hh) *(LAS unsigned*)(yb + (256 * i + 2 * j + 32 * (2 * q + hh)) * 2) = pk[2 * i + hh];
            WAIT_LGKM0();
#pragma unroll
            for (int i = 0; i < 2; ++i) {
                u32x4 v = *(LAS u32x4*)(yb + i * 1024 + l * 16);
                __builtin_amdgcn_raw_buffer_store_b128(v, wsr, WS_Y2 + (ch0 + chn) * (S * 2) + chalf * 2048 + i * 1024 + l * 16, 0, 16);
            }
        }
        STAMP(9);
        BARRIER();
        STAMP(10);
        const char* hbase = p.ws + WS_HF + (size_t)(2 * rt) * NUNIT * 1024;
        const unsigned l16 = l * 16;
#pragma unroll
        for (int i = 0; i < 18; ++i)
            dma16c(hbase + ((w & 1) * NUNIT + 40 + 2 * i + (w >> 1)) * 1024, l16, lds0 + 81920 + (4 * i + w) * 1024);
        WAIT_VM(18);
        if (l == 0) flag_store(p.ws, WS_F3 + ((c * 2 + chalf) * 64 + 2 * rt + chn) * 8, tag);
        BARRIER();
#pragma unroll
        for (int i = 0; i < 5; ++i)
            dma16c(hbase + ((w & 1) * NUNIT + 76 + 2 * i + (w >> 1)) * 1024, l16, lds0 + (4 * i + w) * 1024);
        WAIT_VM(15);
        BARRIER();
        WAIT_VM(5);
        BARRIER();
        WAIT_VM(0);
        BARRIER();
        STAMP(11);
        {
            const int sg = 64 * rt + l, dg = 64 * c + 16 * w;
            u32x4 q0 = *(const u32x4*)(p.q + (size_t)sg * D + dg), q1 = *(const u32x4*)(p.q + (size_t)sg * D + dg + 8);
            float g = __uint_as_float(__builtin_amdgcn_raw_buffer_load_b32(wsr, WS_G + sg * 4, 0, 16));
            poll_flags(p.ws, WS_F3 + ((c * 2 + (rt >> 4)) * 64 + 16 * w + (l & 15)) * 8, tag);
            STAMP(12);
            const unsigned yl = lds0 + L_Y2 + w * 2048;
            for (int i = 0; i < 2; ++i)
                dma16c(p.ws + WS_Y2 + (size_t)(dg + 8 * i) * (S * 2) + rt * 128, (unsigned)((l >> 3) * (S * 2) + (l & 7) * 16), yl + i * 1024);
            WAIT_VM(0);
            const unsigned ya = yl + ((l >> 2) & 3) * 128 + (16 * (l >> 4) + 4 * (l & 3)) * 2;
            unsigned ga[8];
#pragma unroll
            for (int i = 0; i < 4; ++i) {
                s16x4 y = tr_read(ya + i * 512);
                unsigned qa = i < 2 ? q0[2 * i] : q1[2 * i - 4], qb = i < 2 ? q0[2 * i + 1] : q1[2 * i - 3];
                unsigned a0 = pk_bf16(bf_lo(qa) * s2f(y[0]), bf_hi(qa) * s2f(y[1]));
                unsigned a1 = pk_bf16(bf_lo(qb) * s2f(y[2]), bf_hi(qb) * s2f(y[3]));
                ga[2 * i] = pk_bf16(g * bf_lo(a0), g * bf_hi(a0));
                ga[2 * i + 1] = pk_bf16(g * bf_lo(a1), g * bf_hi(a1));
            }
            const unsigned gb = lds0 + L_GA + l * 128;
            *(LAS u32x4*)(gb + 16 * ((2 * w) ^ ((l >> 1) & 7))) = (u32x4){ga[0], ga[1], ga[2], ga[3]};
            *(LAS u32x4*)(gb + 16 * ((2 * w + 1) ^ ((l >> 1) & 7))) = (u32x4){ga[4], ga[5], ga[6], ga[7]};
            if (w == 0) *(LAS float*)(lds0 + L_GA + 8192 + l * 4) = rbf(1.0f - g);
        }
        STAMP(13);
    } else {
        WAIT_VM(22);
        BARRIER();
        STAMP(3);
        {
            const int pc = xw;
#pragma unroll
            for (int chn = 0; chn < 2; ++chn) {
                const char* hb = (const char*)(p.ssm_k + (size_t)(ch0 + chn) * S);
                const unsigned dst = lds0 + L_HC + chn * HC_BYTES + pc * HC_STRIDE;
                if (l < 15 - pc) *(LAS unsigned*)(dst + l * 4) = 0;
                {
                    int li = l < 15 - pc ? 15 - pc : l;
                    if (l >= 15 - pc && l < 16) dma4(hb, (unsigned)((2 * li + 2 * pc - 30) * 2), dst);
                }
#pragma unroll
                for (int i = 0; i < 4; ++i) {
                    int chunk = 4 + 64 * i + l; if (chunk > 258) chunk = 258;
                    dma16(hb, (unsigned)((8 * chunk + 2 * pc - 30) * 2), dst + 64 + i * 1024);
                }
            }
        }
        WAIT_VM(22);
        BARRIER();
        tile_issue(3);
        WAIT_VM(20);
        BARRIER();
        if (w == 4 && l == 0) flag_store(p.ws, WS_F1 + (c * 32 + rt) * 8, tag);
        tile_issue(4);
        WAIT_VM(10);
        BARRIER();
        tile_issue(5);
        WAIT_VM(10);
        BARRIER();
        tile_issue(6);
        WAIT_VM(10);
        BARRIER();
        u64 pv;
        const unsigned f1off = (b == 0) ? (unsigned)(WS_F1 + (xw * 64 + l) * 8) : (unsigned)(WS_F1 + (c * 32 + (l & 31)) * 8);
        asm volatile("global_load_dwordx2 %0, %1, %2 sc1" : "=v"(pv) : "v"(f1off), "s"(p.ws) : "memory");
        tile_issue(7);
        asm volatile("s_waitcnt vmcnt(10)" : "+v"(pv) :: "memory");
        if (!__all(pv == tag)) poll_flags(p.ws, f1off, tag);
        BARRIER();
        STAMP(4);
        if (b == 0 && w == 4 && l == 0) flag_store(p.ws, WS_EP, tag);
        {
            const int chn = xw >> 1, pu = xw & 1;
            const char* ub = p.ws + WS_UR + (size_t)(ch0 + chn) * (UR_PITCH * 2) + (pu ? 0 : 2);
            const unsigned dst = lds0 + L_UC + chn * UC_BYTES + pu * UC_STRIDE;
#pragma unroll
            for (int i = 0; i < 4; ++i) dma16c(ub + i * 1024, (unsigned)(l * 16), dst + i * 1024);
            if (l < 28) dma16c(ub + 4096, (unsigned)(l * 16), dst + 4096);
        }
        const int dh = xw & 1, kh = xw >> 1;
        bf16x8 wf[43];
#pragma unroll
        for (int i = 0; i < 43; ++i)
            wf[i] = __builtin_bit_cast(bf16x8, __builtin_amdgcn_raw_buffer_load_b128(wsr, WS_WF + ((c * NUNIT + kh) * 2 + dh) * 1024 + l * 16, i * 4096, 16));
        WAIT_VM(48);
        BARRIER();
        WAIT_VM(43);
        BARRIER();
        STAMP(6);
        poll_flags(p.ws, WS_F2 + (rt * 32 + (l & 31)) * 8, tag);
#pragma unroll
        for (int i = 0; i < 43; ++i) asm volatile("" : "+v"(wf[i]));
        STAMP(7);
        const char* hbase = p.ws + WS_HF + (size_t)(2 * rt) * NUNIT * 1024;
        const unsigned l16 = l * 16;
#pragma unroll
        for (int i = 0; i < 20; ++i)
            dma16c(hbase + ((xw & 1) * NUNIT + 2 * i + (xw >> 1)) * 1024, l16, lds0 + (4 * i + xw) * 1024);
        WAIT_VM(12);
        STAMP(9);
        BARRIER();
        STAMP(10);
        f32x16 acc[2];
        for (int i = 0; i < 16; ++i) { acc[0][i] = 0.f; acc[1][i] = 0.f; }
        const unsigned hb = lds0 + kh * 2048 + l16;
        auto g2 = [&](int i0, int i1, unsigned base) {
            bf16x8 h0[2], h1[2];
            h0[0] = lds_read128(hb + base); h1[0] = lds_read128(hb + base + 1024);
#pragma unroll
            for (int i = i0; i < i1; ++i) {
                const int cur = (i - i0) & 1;
                if (i + 1 < i1) { h0[cur ^ 1] = lds_read128(hb + base + (i + 1 - i0) * 4096); h1[cur ^ 1] = lds_read128(hb + base + (i + 1 - i0) * 4096 + 1024); }
                SB();
                acc[0] = __builtin_amdgcn_mfma_f32_32x32x16_bf16(wf[i], h0[cur], acc[0], 0, 0, 0);
                acc[1] = __builtin_amdgcn_mfma_f32_32x32x16_bf16(wf[i], h1[cur], acc[1], 0, 0, 0);
                SB();
            }
        };
        g2(0, 8, 0);
        WAIT_VM(0);
        BARRIER();
        g2(8, 20, 32768);
        BARRIER();
        g2(20, 29, 81920);
        BARRIER();
        g2(29, 38, 81920 + 36864);
        BARRIER();
        STAMP(11);
        g2(38, 43, 0);
#pragma unroll
        for (int sh = 0; sh < 2; ++sh) {
            const int s = 32 * sh + (l & 31);
#pragma unroll
            for (int a = 0; a < 4; ++a) {
                const int chunk = 8 * dh + 4 * (a >> 1) + 2 * (l >> 5) + (a & 1);
                *(LAS f32x4*)(lds0 + L_P + kh * 16384 + s * 256 + 16 * (chunk ^ (s & 15))) =
                    (f32x4){acc[sh][4 * a], acc[sh][4 * a + 1], acc[sh][4 * a + 2], acc[sh][4 * a + 3]};
            }
        }
        STAMP(13);
    }
    BARRIER();
    STAMP(14);
    {
        const int s = tid >> 3, ck = tid & 7;
        const unsigned pa = lds0 + L_P + s * 256;
        f32x4 a0 = *(LAS f32x4*)(pa + 16 * ((2 * ck) ^ (s & 15))), a1 = *(LAS f32x4*)(pa + 16 * ((2 * ck + 1) ^ (s & 15)));
        f32x4 b0 = *(LAS f32x4*)(pa + 16384 + 16 * ((2 * ck) ^ (s & 15))), b1 = *(LAS f32x4*)(pa + 16384 + 16 * ((2 * ck + 1) ^ (s & 15)));
        u32x4 ga = *(LAS u32x4*)(lds0 + L_GA + s * 128 + 16 * (ck ^ ((s >> 1) & 7)));
        float omg = *(LAS float*)(lds0 + L_GA + 8192 + s * 4);
        a0 += b0; a1 += b1;
        unsigned o[4];
#pragma unroll
        for (int i = 0; i < 4; ++i) {
            unsigned bv = i < 2 ? pk_bf16(a0[2 * i], a0[2 * i + 1]) : pk_bf16(a1[2 * i - 4], a1[2 * i - 3]);
            unsigned t2 = pk_bf16(omg * bf_lo(bv), omg * bf_hi(bv));
            o[i] = pk_bf16(bf_lo(ga[i]) + bf_lo(t2), bf_hi(ga[i]) + bf_hi(t2));
        }
        *(u32x4*)(p.out + (size_t)(64 * rt + s) * D + 64 * c + 8 * ck) = (u32x4){o[0], o[1], o[2], o[3]};
    }
    STAMP(15);
}

extern "C" void kernel_launch(void* const* d_in, const int* in_sizes, int n_in,
                              void* d_out, int out_size, void* d_ws, size_t ws_size,
                              hipStream_t stream) {
    Params p;
    p.q = (const u16*)d_in[0]; p.k = (const u16*)d_in[1]; p.v = (const u16*)d_in[2];
    p.shift_k = (const u16*)d_in[3]; p.ssm_k = (const u16*)d_in[4]; p.ssm_D = (const u16*)d_in[5];
    p.wg = (const u16*)d_in[7]; p.wu = (const u16*)d_in[8]; p.wd = (const u16*)d_in[9]; p.gw = (const u16*)d_in[10];
    p.out = (u16*)d_out; p.ws = (char*)d_ws;
    (void)hipFuncSetAttribute((const void*)fused_kernel, hipFuncAttributeMaxDynamicSharedMemorySize, LDS_BYTES);
    fused_kernel<<<256, 512, LDS_BYTES, stream>>>(p);
}
```

```cpp
#include <hip/hip_runtime.h>
#include <hip/hip_bf16.h>
#include <stdint.h>

typedef unsigned short u16;
typedef unsigned long long u64;
typedef short s16x4 __attribute__((ext_vector_type(4)));
typedef __bf16 bf16x8 __attribute__((ext_vector_type(8)));
typedef __bf16 bf16x2 __attribute__((ext_vector_type(2)));
typedef float f32x2 __attribute__((ext_vector_type(2)));
typedef float f32x4 __attribute__((ext_vector_type(4)));
typedef float f32x16 __attribute__((ext_vector_type(16)));
typedef unsigned u32x2 __attribute__((ext_vector_type(2)));
typedef unsigned u32x4 __attribute__((ext_vector_type(4)));
#define LAS __attribute__((address_space(3)))
#define GAS __attribute__((address_space(1)))
#define CAS __attribute__((address_space(4)))

constexpr int S = 2048, D = 512, F = 1376;
constexpr int NUNIT = F / 16;

constexpr unsigned WS_EP = 0;
constexpr unsigned WS_F1 = 4096;
constexpr unsigned WS_F2 = WS_F1 + 4096;
constexpr unsigned WS_F3 = WS_F2 + 8192;
constexpr unsigned WS_G  = 32768;
constexpr unsigned WS_UR = 65536;
constexpr unsigned WS_HF = WS_UR + D * S * 2;
constexpr unsigned WS_Y2 = WS_HF + 64 * NUNIT * 1024;
constexpr unsigned WS_END = WS_Y2 + D * S * 2;

#define NTS " nt"
#define NTA 2
#define HFLAG_AT 0
#define ROT 5
#define HROT 0
#define HAHEAD 4
#define PRIO_C 2
#define PRIO_A 1
#define F3_AT 10
#define CODEPF 1
#define SLP 1
#define PUBAT 16
#define WD_START 5
#define BSTEP 2
#define PAUSE 1
#define AUXFIRST 1
#define THR 12
#define PSLP 8
#define NARROW 1
constexpr unsigned G1_STAGE = 41024, G1_WG = 16384, G1_WU = 16384 + 12288 + 32;
constexpr unsigned HC_STRIDE = 282 * 16, HC_BYTES = 4 * HC_STRIDE;
constexpr unsigned UC_STRIDE = 264 * 16, UC_BYTES = 2 * UC_STRIDE;
constexpr unsigned L_HC = 0;
constexpr unsigned L_RED = G1_STAGE;
constexpr unsigned L_UC = 2 * G1_STAGE;
constexpr unsigned L_WB = L_UC + 2 * UC_BYTES;
constexpr unsigned L_WA = L_WB + 4 * 6144;
constexpr unsigned L_SCR = 3 * G1_STAGE;
constexpr unsigned SC_K = 0, SC_V = 5120, SC_X = 9216, SC_GW = 13312, SC_SK = 14336, SC_BYTES = 15360;
constexpr unsigned L_DUMP = 161792, L_OMG = 162048;
constexpr unsigned L_FLAG = 163840 - 128;
constexpr unsigned LDS_BYTES = 163840;
constexpr int FL_FULL = 0, FL_UOK = 2, FL_FREE = 4, FL_EXCH = 8, FL_P = 12, FL_ST = 16, FL_F2 = 22, FL_F3 = 23, FL_PROG = 24, FL_PAUSE = 25, FL_F1 = 26;
constexpr int NWR = 15;

__device__ __forceinline__ float bf_lo(unsigned x) { return __uint_as_float(x << 16); }
__device__ __forceinline__ float bf_hi(unsigned x) { return __uint_as_float(x & 0xffff0000u); }
__device__ __forceinline__ unsigned pk_bf16(float a, float b) {
    f32x2 t = {a, b}; bf16x2 p = __builtin_convertvector(t, bf16x2); return __builtin_bit_cast(unsigned, p);
}
__device__ __forceinline__ float rbf(float a) { return bf_lo(pk_bf16(a, a)); }

__device__ __forceinline__ void dma16(const void* sbase, unsigned voff, unsigned lds) {
    asm volatile("s_mov_b32 m0, %2\n\ts_nop 0\n\tglobal_load_lds_dwordx4 %0, %1" :: "v"(voff), "s"(sbase), "s"(lds) : "memory");
}
__device__ __forceinline__ void dma16n(const void* sbase, unsigned voff, unsigned lds) {
    asm volatile("s_mov_b32 m0, %2\n\ts_nop 0\n\tglobal_load_lds_dwordx4 %0, %1" NTS :: "v"(voff), "s"(sbase), "s"(lds) : "memory");
}
__device__ __forceinline__ void dma4n(const void* sbase, unsigned voff, unsigned lds) {
    asm volatile("s_mov_b32 m0, %2\n\ts_nop 0\n\tglobal_load_lds_dword %0, %1" NTS :: "v"(voff), "s"(sbase), "s"(lds) : "memory");
}
__device__ __forceinline__ float fmul1(float a, float b) { float r; asm("v_mul_f32 %0, %1, %2" : "=v"(r) : "v"(a), "v"(b)); return r; }
__device__ __forceinline__ float ffma1(float a, float b, float c) { float r; asm("v_fma_f32 %0, %1, %2, %3" : "=v"(r) : "v"(a), "v"(b), "v"(c)); return r; }
template <int CTRL> __device__ __forceinline__ float dpp_add(float a) {
    return a + __builtin_bit_cast(float, __builtin_amdgcn_update_dpp(0, __builtin_bit_cast(int, a), CTRL, 0xf, 0xf, true));
}
__device__ __forceinline__ float fadd1(float a, float b) { float r; asm("v_add_f32 %0, %1, %2" : "=v"(r) : "v"(a), "v"(b)); return r; }
__device__ __forceinline__ void dma16c(const void* sbase, unsigned voff, unsigned lds) {
    asm volatile("s_mov_b32 m0, %2\n\ts_nop 0\n\tglobal_load_lds_dwordx4 %0, %1 sc1" :: "v"(voff), "s"(sbase), "s"(lds) : "memory");
}
__device__ __forceinline__ void dma4(const void* sbase, unsigned voff, unsigned lds) {
    asm volatile("s_mov_b32 m0, %2\n\ts_nop 0\n\tglobal_load_lds_dword %0, %1" :: "v"(voff), "s"(sbase), "s"(lds) : "memory");
}
#define DMA_G8(SC) \
    "s_mov_b32 m0, %9\n\ts_nop 0\n\t" \
    "global_load_lds_dwordx4 %0, %8 offset:-4096" SC "\n\tglobal_load_lds_dwordx4 %1, %8 offset:-3072" SC "\n\t" \
    "global_load_lds_dwordx4 %2, %8 offset:-2048" SC "\n\tglobal_load_lds_dwordx4 %3, %8 offset:-1024" SC "\n\t" \
    "global_load_lds_dwordx4 %4, %8" SC "\n\tglobal_load_lds_dwordx4 %5, %8 offset:1024" SC "\n\t" \
    "global_load_lds_dwordx4 %6, %8 offset:2048" SC "\n\tglobal_load_lds_dwordx4 %7, %8 offset:3072" SC
__device__ __forceinline__ void dma16x8(const void* sbase, const unsigned* v, unsigned lds) {
    asm volatile(DMA_G8("") :: "v"(v[0]), "v"(v[1]), "v"(v[2]), "v"(v[3]), "v"(v[4]), "v"(v[5]), "v"(v[6]), "v"(v[7]), "s"(sbase), "s"(lds + 4096) : "memory");
}
#define DMA_G4(SC) \
    "s_mov_b32 m0, %5\n\ts_nop 0\n\t" \
    "global_load_lds_dwordx4 %0, %4" SC "\n\tglobal_load_lds_dwordx4 %1, %4 offset:1024" SC "\n\t" \
    "global_load_lds_dwordx4 %2, %4 offset:2048" SC "\n\tglobal_load_lds_dwordx4 %3, %4 offset:3072" SC
__device__ __forceinline__ void dma16x4(const void* sbase, unsigned v0, unsigned v1, unsigned v2, unsigned v3, unsigned lds) {
    asm volatile(DMA_G4("") :: "v"(v0), "v"(v1), "v"(v2), "v"(v3), "s"(sbase), "s"(lds) : "memory");
}
__device__ __forceinline__ void dma16x4n(const void* sbase, unsigned v0, unsigned v1, unsigned v2, unsigned v3, unsigned lds) {
    asm volatile(DMA_G4(NTS) :: "v"(v0), "v"(v1), "v"(v2), "v"(v3), "s"(sbase), "s"(lds) : "memory");
}
__device__ __forceinline__ void dma16x4c(const void* sbase, unsigned v0, unsigned v1, unsigned v2, unsigned v3, unsigned lds) {
    asm volatile(DMA_G4(" sc1") :: "v"(v0), "v"(v1), "v"(v2), "v"(v3), "s"(sbase), "s"(lds) : "memory");
}
__device__ __forceinline__ void dma16x2(const void* sbase, unsigned v0, unsigned v1, unsigned lds) {
    asm volatile("s_mov_b32 m0, %3\n\ts_nop 0\n\tglobal_load_lds_dwordx4 %0, %2\n\tglobal_load_lds_dwordx4 %1, %2 offset:1024" :: "v"(v0), "v"(v1), "s"(sbase), "s"(lds) : "memory");
}
__device__ __forceinline__ void dma16x2c(const void* sbase, unsigned v0, unsigned v1, unsigned lds) {
    asm volatile("s_mov_b32 m0, %3\n\ts_nop 0\n\tglobal_load_lds_dwordx4 %0, %2 sc1\n\tglobal_load_lds_dwordx4 %1, %2 offset:1024 sc1" :: "v"(v0), "v"(v1), "s"(sbase), "s"(lds) : "memory");
}
#define WAIT_VM(n) asm volatile("s_waitcnt vmcnt(" #n ")" ::: "memory")
#define WAITN(n) asm volatile("s_waitcnt vmcnt(%0)" :: "i"(n) : "memory")
#define WAIT_LGKM0() asm volatile("s_waitcnt lgkmcnt(0)" ::: "memory")
#define BARRIER() asm volatile("s_waitcnt lgkmcnt(0)\n\ts_barrier" ::: "memory")
#define SB() __builtin_amdgcn_sched_barrier(0)

__device__ __forceinline__ s16x4 tr_read(unsigned addr) { return __builtin_amdgcn_ds_read_tr16_b64_v4i16((LAS s16x4*)addr); }
__device__ __forceinline__ bf16x8 cat8(s16x4 a, s16x4 b) {
    return __builtin_bit_cast(bf16x8, __builtin_shufflevector(a, b, 0, 1, 2, 3, 4, 5, 6, 7));
}
__device__ __forceinline__ bf16x8 lds_read128(unsigned addr) { return *(LAS bf16x8*)addr; }
__device__ __forceinline__ float s2f(short x) { return __uint_as_float((unsigned)(u16)x << 16); }

__device__ __forceinline__ unsigned gelu_mul2(f32x2 g, f32x2 u) {
    const float c1 = -2.0f * 0.7978845608028654f * 1.4426950408889634f;
    const float c2 = c1 * 0.044715f;
    f32x2 t = g * g;
    t = __builtin_elementwise_fma(t, (f32x2){c2, c2}, (f32x2){c1, c1});
    t = t * g;
    f32x2 e = {__builtin_amdgcn_exp2f(t[0]), __builtin_amdgcn_exp2f(t[1])};
    e = e + (f32x2){1.0f, 1.0f};
    f32x2 r = {__builtin_amdgcn_rcpf(e[0]), __builtin_amdgcn_rcpf(e[1])};
    f32x2 h = (g * u) * r;
    return __builtin_bit_cast(unsigned, __builtin_convertvector(h, bf16x2));
}

#define STAMP_DECL
#define ACC_BEGIN()
#define ACC_END(i)
#define STAMP(i)
#define STAMP_FLUSH(kid)

struct Params {
    const u16 *q, *k, *v, *shift_k, *ssm_k, *ssm_D, *wg, *wu, *wd, *gw;
    u16* out;
    char* ws;
};

#define SPIN_CAP (1 << 18)
__device__ __forceinline__ void poll_flags(const char* ws, unsigned voff, u64 tag) {
#pragma unroll 1
    for (int it = 0; it < SPIN_CAP; ++it) {
        u64 v;
        asm volatile("global_load_dwordx2 %0, %1, %2 sc1\n\ts_waitcnt vmcnt(0)" : "=&v"(v) : "v"(voff), "s"(ws) : "memory");
        if (__all(v == tag)) break;
        __builtin_amdgcn_s_sleep(PSLP);
    }
}
__device__ __forceinline__ void flag_store(char* ws, unsigned off, u64 tag) {
    __hip_atomic_store((GAS u64*)(ws + off), tag, __ATOMIC_RELAXED, __HIP_MEMORY_SCOPE_AGENT);
}
template <int N> struct IC { static constexpr int v = N; };

__global__ __launch_bounds__(512, 2) void fused_kernel(Params p) {
    extern __shared__ __attribute__((aligned(16))) char smem[];
    const unsigned lds0 = (unsigned)(size_t)(LAS char*)smem;
    const int b = blockIdx.x, xcd = b & 7, r = b >> 3;
    const int tid = threadIdx.x, l = tid & 63;
    const int w = __builtin_amdgcn_readfirstlane(tid >> 6);
    const int rt = 4 * xcd + (r >> 3), c = r & 7;
    const int mt = 8 * (xcd >> 2) + (r >> 2), cj = xcd & 3, ci = r & 3, ct = 4 * cj + ci;
    const int st32 = 11 * cj + 3 * ci - (ct == 15);
    const bool narrow = ci == 3 || ct == 14;
    const int ch0 = 64 * c + 2 * rt;
    u64 tag = *(const CAS u64*)(p.ws + WS_EP) + 1;
    STAMP_DECL STAMP(0);
    const __amdgpu_buffer_rsrc_t wsr = __builtin_amdgcn_make_buffer_rsrc(p.ws, 0, WS_END, 0x00020000);
    const int xw = w & 3;
    const unsigned l16 = l * 16;
    const unsigned scr = lds0 + L_SCR;
    volatile LAS unsigned* lflag = (volatile LAS unsigned*)(lds0 + L_FLAG);
    auto wait_min4 = [&](int idx, unsigned n) {
#pragma unroll 1
        for (int it = 0; it < SPIN_CAP; ++it) {
            u32x4 f = *(volatile LAS u32x4*)(lds0 + L_FLAG + idx * 4);
            if (__builtin_amdgcn_readfirstlane(min(min(f[0], f[1]), min(f[2], f[3]))) >= n) break;
            __builtin_amdgcn_s_sleep(SLP);
        }
        asm volatile("" ::: "memory");
    };
    auto wait_word = [&](int idx) {
#pragma unroll 1
        for (int it = 0; it < SPIN_CAP; ++it) { if (__builtin_amdgcn_readfirstlane(lflag[idx])) break; __builtin_amdgcn_s_sleep(1); }
        asm volatile("" ::: "memory");
    };

    if (w < 4) {
        __builtin_amdgcn_s_setprio(PRIO_C);
        unsigned dpair = *(const CAS unsigned*)(p.ssm_D + ch0);
        if (w == 0 && l < 32) lflag[l] = 0;
        const int wm = w & 1, wn = w >> 1;
        const int nj = (NARROW && narrow) ? 2 : 3;
        unsigned xa[2][4], wa[2];
        {
            const int sr = l & 31, hi = l >> 5;
            for (int ks = 0; ks < 4; ++ks) { xa[0][ks] = lds0 + (64 * wm + sr) * 128 + 16 * ((2 * ks + hi) ^ ((sr >> 1) & 7)); xa[1][ks] = xa[0][ks] + 2 * G1_STAGE; }
            const int g4 = l >> 4, qq = (l >> 2) & 3, pp = l & 3;
            wa[0] = lds0 + ((pp >> 1) ? G1_WU : G1_WG) + (8 * (g4 >> 1) + qq) * 192 + (nj * wn) * 32 + (2 * (pp & 1) + (g4 & 1)) * 8;
            wa[1] = wa[0] + 2 * G1_STAGE;
            for (int ks = 0; ks < 4; ++ks) asm volatile("" : "+v"(xa[1][ks]));
            asm volatile("" : "+v"(wa[1]));
        }
        asm volatile("" : "+s"(tag), "+s"(dpair));
        BARRIER();
        auto wait_full = [&](unsigned n) {
#pragma unroll 1
            for (int it = 0; it < SPIN_CAP; ++it) {
                u32x2 f = *(volatile LAS u32x2*)(lds0 + L_FLAG + FL_FULL * 4);
                if (__builtin_amdgcn_readfirstlane(min(f[0], f[1])) >= n) break;
            }
            asm volatile("" ::: "memory");
        };
        auto gemm1 = [&](auto njc) {
            constexpr int NJ = decltype(njc)::v;
            f32x16 acc[2][NJ];
            for (int s = 0; s < 2; ++s) for (int j = 0; j < NJ; ++j) for (int i = 0; i < 16; ++i) acc[s][j][i] = 0.f;
            bf16x8 xb[2][2]; s16x4 al[2][NJ], ah[2][NJ];
            wait_full(1);
            STAMP(1);
            xb[0][0] = lds_read128(xa[0][0]); al[0][0] = tr_read(wa[0]); ah[0][0] = tr_read(wa[0] + 768); xb[0][1] = lds_read128(xa[0][0] + 4096);
            for (int j = 1; j < NJ; ++j) { al[0][j] = tr_read(wa[0] + j * 32); ah[0][j] = tr_read(wa[0] + j * 32 + 768); }
            SB();
#pragma unroll
            for (int kt = 0; kt < 8; ++kt) {
                u32x2 fl; unsigned fm = 0, fs = 0;
#pragma unroll
                for (int ks = 0; ks < 4; ++ks) {
                    const int cur = ks & 1, nx = cur ^ 1, nks = (ks + 1) & 3;
                    const int nkt = ks == 3 ? kt + 1 : kt, st = nkt % 3;
                    const bool rd = nkt < 8;
                    const unsigned xr = xa[st == 2][nks] + (st == 1 ? G1_STAGE : 0), wr = wa[st == 2] + (st == 1 ? G1_STAGE : 0) + nks * 3072;
#pragma unroll
                    for (int i = 0; i < 2 * NJ; ++i) {
                        const int j = i >> 1, s = i & 1;
                        acc[s][j] = __builtin_amdgcn_mfma_f32_32x32x16_bf16(cat8(al[cur][j], ah[cur][j]), xb[cur][s], acc[s][j], 0, 0, 0);
                        SB();
                        if (ks == 3 && i == 0) {
                            asm volatile("" ::: "memory"); lflag[FL_FREE + w] = kt + 1;
                            if (rd && __builtin_expect(fs < (unsigned)(kt + 2), 0)) { ACC_BEGIN(); wait_full(kt + 2); ACC_END(20); }
                        }
                        if (rd) {
                            if (i == 0) { xb[nx][0] = lds_read128(xr); al[nx][0] = tr_read(wr); }
                            if (i == 1) { ah[nx][0] = tr_read(wr + 768); xb[nx][1] = lds_read128(xr + 4096); }
                            if (i >= 2) { if (i & 1) ah[nx][j] = tr_read(wr + j * 32 + 768); else al[nx][j] = tr_read(wr + j * 32); }
                        }
                        if (kt < 7) {
                            if (ks == 1 && i == 2 * NJ - 1) fl = *(volatile LAS u32x2*)(lds0 + L_FLAG + FL_FULL * 4);
                            if (ks == 2 && i == 2 * NJ - 2) fm = min(fl[0], fl[1]);
                            if (ks == 2 && i == 2 * NJ - 1) fs = __builtin_amdgcn_readfirstlane(fm);
                        }
                        SB();
                    }
                }
            }
            STAMP(9);
#pragma unroll
            for (int j = 0; j < NJ; ++j) {
#pragma unroll
                for (int s = 0; s < 2; ++s) {
                    const f32x16 v = acc[s][j];
                    u32x4 o = {gelu_mul2((f32x2){v[0], v[1]}, (f32x2){v[4], v[5]}), gelu_mul2((f32x2){v[2], v[3]}, (f32x2){v[6], v[7]}),
                               gelu_mul2((f32x2){v[8], v[9]}, (f32x2){v[12], v[13]}), gelu_mul2((f32x2){v[10], v[11]}, (f32x2){v[14], v[15]})};
                    if (j == NJ - 1 && s == 0) lflag[FL_PAUSE] = 1;
                    if (NARROW || !narrow || NJ * wn + j < 4)
                        __builtin_amdgcn_raw_buffer_store_b128(o, wsr, WS_HF + ((4 * mt + 2 * wm + s) * NUNIT + 2 * st32 + NJ * wn + j) * 1024 + l16, 0, 16);
                }
            }
        };
        if (NARROW && narrow) gemm1(IC<2>{}); else gemm1(IC<3>{});
        STAMP(10);
        const int chn = w >> 1, chalf = w & 1;
        const unsigned f2off = WS_F2 + ((2 * mt + wm) * 32 + wn * 16 + ct) * 8;
        bool hpub = false;
        if (!__builtin_amdgcn_readfirstlane(lflag[FL_UOK + chn])) {
            WAIT_VM(0);
            if (l == 0) flag_store(p.ws, f2off, tag);
            hpub = true;
            lflag[FL_FREE + w] = 9;
            wait_word(FL_UOK + chn);
        }
        asm volatile("" ::: "memory");
        STAMP(11);
        f32x4 ca[8];
        {
            const int i16 = l & 15, g = l >> 4;
            const unsigned ha = lds0 + L_HC + chn * HC_BYTES + (i16 & 3) * HC_STRIDE + (8 * (i16 >> 2) + g) * 16 + chalf * 256;
            const unsigned ua = lds0 + L_UC + chn * UC_BYTES + ((i16 & 1) ^ 1) * UC_STRIDE + (7 - (i16 >> 1) + g) * 16 + chalf * 256;
            for (int n = 0; n < 8; ++n) ca[n] = (f32x4){0, 0, 0, 0};
            bf16x8 uf[2][8], hf[2][8];
            hf[0][0] = lds_read128(ha);
#pragma unroll
            for (int e = 7; e >= 0; --e) uf[0][e] = lds_read128(ua + (7 - e) * 512);
#pragma unroll
            for (int a = 1; a < 8; ++a) hf[0][a] = lds_read128(ha + a * 512);
            SB();
#pragma unroll
            for (int cc = 0; cc < 4; ++cc) {
                const int cur = cc & 1, nx = cur ^ 1;
                int cnt = 0;
#pragma unroll
                for (int a = 0; a < 8; ++a) {
#pragma unroll
                    for (int e = 7 - a; e >= 0; --e) {
                        ca[a + e] = __builtin_amdgcn_mfma_f32_16x16x32_bf16(hf[cur][a], uf[cur][e], ca[a + e], 0, 0, 0);
                        SB();
                        if (cc < 3 && (cnt & 1) == 0 && cnt < 32) {
                            const int ri = cnt >> 1;
                            if (ri == 0) hf[nx][0] = lds_read128(ha + (cc + 1) * 64);
                            else if (ri <= 8) uf[nx][8 - ri] = lds_read128(ua + (cc + 1) * 64 + (ri - 1) * 512);
                            else hf[nx][ri - 8] = lds_read128(ha + (cc + 1) * 64 + (ri - 8) * 512);
                            SB();
                        }
                        ++cnt;
                    }
                }
                if (cc == HFLAG_AT) {
                    WAIT_VM(0);
                    if (l == 0 && !hpub) flag_store(p.ws, f2off, tag);
                    lflag[FL_FREE + w] = 9;
                    STAMP(12);
                    SB();
                }
            }
        }
        STAMP(13);
        const unsigned red = lds0 + L_RED;
        wait_min4(FL_FREE, 8);
        {
            u32x2 uu[4];
#pragma unroll
            for (int i = 0; i < 4; ++i) uu[i] = *(LAS u32x2*)(lds0 + L_UC + chn * UC_BYTES + (2044 - 256 * (4 * chalf + i) - 4 * l) * 2);
            if (chalf) {
#pragma unroll
                for (int i = 0; i < 4; ++i) { *(LAS f32x4*)(red + w * 4096 + i * 1024 + l16) = ca[i]; ca[i] = ca[4 + i]; }
            } else {
#pragma unroll
                for (int i = 0; i < 4; ++i) *(LAS f32x4*)(red + w * 4096 + i * 1024 + l16) = ca[4 + i];
            }
            asm volatile("" ::: "memory");
            lflag[FL_EXCH + w] = 1;
            const float Dd = chn ? bf_hi(dpair) : bf_lo(dpair);
            unsigned dd[8];
#pragma unroll
            for (int i = 0; i < 4; ++i) {
                dd[2 * i] = pk_bf16(fmul1(bf_hi(uu[i][1]), Dd), fmul1(bf_lo(uu[i][1]), Dd));
                dd[2 * i + 1] = pk_bf16(fmul1(bf_hi(uu[i][0]), Dd), fmul1(bf_lo(uu[i][0]), Dd));
            }
#pragma unroll 1
            for (int it = 0; it < SPIN_CAP; ++it) { if (__builtin_amdgcn_readfirstlane(lflag[FL_EXCH + (w ^ 1)])) break; }
            asm volatile("" ::: "memory");
#pragma unroll
            for (int i = 0; i < 4; ++i) {
                f32x4 o = *(LAS f32x4*)(red + (w ^ 1) * 4096 + i * 1024 + l16);
                unsigned y0 = pk_bf16(fadd1(o[0], ca[i][0]), fadd1(o[1], ca[i][1])), y1 = pk_bf16(fadd1(o[2], ca[i][2]), fadd1(o[3], ca[i][3]));
                u32x2 r2 = {pk_bf16(fadd1(bf_lo(y0), bf_lo(dd[2 * i])), fadd1(bf_hi(y0), bf_hi(dd[2 * i]))),
                            pk_bf16(fadd1(bf_lo(y1), bf_lo(dd[2 * i + 1])), fadd1(bf_hi(y1), bf_hi(dd[2 * i + 1])))};
                __builtin_amdgcn_raw_buffer_store_b64(r2, wsr, WS_Y2 + (ch0 + chn) * (S * 2) + chalf * 2048 + i * 512 + l * 8, 0, 16);
            }
        }
        STAMP(14);
        WAIT_VM(0);
        if (l == 0) flag_store(p.ws, WS_F3 + ((c * 2 + chalf) * 64 + 2 * rt + chn) * 8, tag);
        STAMP(15);
        {
            const int sg = 64 * rt + l, dg = 64 * c + 16 * w;
            u32x4 q0 = *(const u32x4*)(p.q + (size_t)sg * D + dg), q1 = *(const u32x4*)(p.q + (size_t)sg * D + dg + 8);
            float g = __uint_as_float(__builtin_amdgcn_raw_buffer_load_b32(wsr, WS_G + sg * 4, 0, 16));
            if (w == 0) {
                wait_word(FL_PROG);
                poll_flags(p.ws, WS_F3 + ((c * 2 + (rt >> 4)) * 64 + l) * 8, tag);
                lflag[FL_F3] = 1;
            } else wait_word(FL_F3);
            STAMP(16);
            const unsigned yl = red + (w ^ 1) * 4096;
            const char* ysrc = p.ws + WS_Y2 + (size_t)dg * (S * 2) + rt * 128;
            const unsigned yoff = (unsigned)((l >> 3) * (S * 2) + (l & 7) * 16);
            dma16x2c(ysrc, yoff, yoff + 8 * S * 2 - 1024, yl);
            WAIT_VM(0);
            STAMP(17);
            const unsigned ya = yl + ((l >> 2) & 3) * 128 + (16 * (l >> 4) + 4 * (l & 3)) * 2;
            unsigned ga[8];
#pragma unroll
            for (int i = 0; i < 4; ++i) {
                s16x4 y = tr_read(ya + i * 512);
                unsigned qa = i < 2 ? q0[2 * i] : q1[2 * i - 4], qb = i < 2 ? q0[2 * i + 1] : q1[2 * i - 3];
                unsigned a0 = pk_bf16(fmul1(bf_lo(qa), s2f(y[0])), fmul1(bf_hi(qa), s2f(y[1])));
                unsigned a1 = pk_bf16(fmul1(bf_lo(qb), s2f(y[2])), fmul1(bf_hi(qb), s2f(y[3])));
                ga[2 * i] = pk_bf16(fmul1(g, bf_lo(a0)), fmul1(g, bf_hi(a0)));
                ga[2 * i + 1] = pk_bf16(fmul1(g, bf_lo(a1)), fmul1(g, bf_hi(a1)));
            }
            *(LAS u32x4*)(yl + 2048 + l16) = (u32x4){ga[0], ga[1], ga[2], ga[3]};
            *(LAS u32x4*)(yl + 3072 + l16) = (u32x4){ga[4], ga[5], ga[6], ga[7]};
            if (w == 0) *(LAS float*)(lds0 + L_OMG + l * 4) = rbf(1.0f - g);
        }
        STAMP(21);
        BARRIER();
        asm volatile("fk_end_c:");
    } else {
        const bool aux = xw >= 2;
        const int aw = xw & 1;
        u32x4 hd[4]; u32x2 hp[4];
        if (!aux) {
            unsigned xo[8], wo[12];
#pragma unroll
            for (int j = 0; j < 3; ++j) {
                int n = 64 * j + l, row = n / 12, cc = n % 12;
                if (narrow && cc >= 8) cc -= 4;
                unsigned o = (unsigned)(row * (F * 2) + cc * 16);
#pragma unroll
                for (int g = 0; g < 4; ++g) { const int jj = 3 * g + j; wo[jj] = o + g * 16 * (F * 2) - (jj & 3) * 1024; }
            }
#pragma unroll
            for (int j = 0; j < 8; ++j) {
                int row = 8 * j + (l >> 3);
                xo[j] = (unsigned)(row * 1024 + (((l & 7) ^ ((row >> 1) & 7)) * 16) - (j & 3) * 1024);
            }
            const char* xsrc = (const char*)p.q + (size_t)(128 * mt + 64 * xw) * 1024;
            const char* wsrc = (const char*)(xw ? p.wu : p.wg) + st32 * 64;
            const int rot = ROT == 1 ? (r >> 2) + 2 * (r & 3) : ROT == 5 ? 4 * (((r >> 2) + (r & 3)) & 1) + 2 * (xcd >> 2) + (xcd & 3) : 0;
            unsigned so = 0;
#pragma unroll 1
            for (int i = 0; i < 8; ++i) {
                const int kt = (i + rot) & 7;
                const char* xb = xsrc + kt * 128;
                const char* wb = wsrc + (size_t)kt * 64 * (F * 2);
                const unsigned xl = lds0 + so + xw * 8192, wl = lds0 + so + (xw ? G1_WU : G1_WG);
                if (i >= 3) {
                    u32x4 f = *(volatile LAS u32x4*)(lds0 + L_FLAG + FL_FREE * 4);
                    if (__builtin_amdgcn_readfirstlane(min(min(f[0], f[1]), min(f[2], f[3]))) < (unsigned)(i - 2)) {
                        ACC_BEGIN();
                        WAIT_VM(0);
                        lflag[FL_FULL + xw] = i;
                        wait_min4(FL_FREE, i - 2);
                        ACC_END(20);
                    }
                }
                dma16x4(wb, wo[0], wo[1], wo[2], wo[3], wl);
                WAITN(THR);
                dma16x4(xb, xo[0], xo[1], xo[2], xo[3], xl);
                if (i == 0) { asm volatile("" : "+s"(tag)); BARRIER(); }
                WAITN(THR);
                if (THR == 8 && i > 0) { lflag[FL_FULL + xw] = i; STAMP(i == 6 ? 13 : i == 7 ? 14 : i); }
                dma16x4(wb, wo[4], wo[5], wo[6], wo[7], wl + 4096);
                WAITN(THR);
                if (THR == 12 && i > 0) { lflag[FL_FULL + xw] = i; STAMP(i == 6 ? 13 : i == 7 ? 14 : i); }
                dma16x4(xb, xo[4], xo[5], xo[6], xo[7], xl + 4096);
                WAITN(THR);
                if (THR == 16 && i > 0) { lflag[FL_FULL + xw] = i; STAMP(i == 6 ? 13 : i == 7 ? 14 : i); }
                dma16x4(wb, wo[8], wo[9], wo[10], wo[11], wl + 8192);
                WAITN(THR);
                so = so == 2 * G1_STAGE ? 0 : so + G1_STAGE;
            }
            wait_word(FL_ST); wait_word(FL_ST + 1);
        } else {
            __builtin_amdgcn_s_setprio(PRIO_A);
            const unsigned sc = scr + aw * SC_BYTES;
            const int grow = 64 * rt + 8 * c + 4 * aw, rb = 64 * rt + 32 * aw;
            const unsigned vo = (unsigned)((rb + (l >> 3)) * 1024 + c * 128 + (l & 7) * 16);
            dma16x4n(p.k, rb == 0 && l < 32 ? vo : vo - 4096, vo - 4096 + 7168, vo - 4096 + 2 * 7168, vo - 4096 + 3 * 7168, sc + SC_K);
            dma16n(p.k, vo + (28 - ((l >> 3) & 4)) * 1024, sc + SC_K + 4096);
            dma16x4n(p.v, vo, vo + 7168, vo + 2 * 7168, vo + 3 * 7168, sc + SC_V);
            dma16(p.shift_k, (unsigned)(c * 512 + (l & 31) * 16), sc + SC_SK);
            dma16(p.gw, l16, sc + SC_GW);
            { const unsigned o = (unsigned)(grow * 1024) + l16; dma16x4(p.q, o, o, o, o, sc + SC_X); }
            {
                const __amdgpu_buffer_rsrc_t hr = __builtin_amdgcn_make_buffer_rsrc((void*)(p.ssm_k + (size_t)(ch0 + aw) * S), 0, S * 2, 0x00020000);
#pragma unroll
                for (int i = 0; i < 4; ++i) {
                    hd[i] = __builtin_amdgcn_raw_buffer_load_b128(hr, l16 + i * 1024, 0, NTA);
                    hp[i] = __builtin_amdgcn_raw_buffer_load_b64(hr, l16 + i * 1024 - 8, 0, NTA);
                }
            }
            if (aw == 0) {
                dma4(p.wd, (unsigned)((b * 43 + (l < 43 ? l : 42)) * 128), lds0 + L_DUMP);
            } else {
                u64 pc; unsigned n1, n2, n0;
                asm volatile("s_getpc_b64 %0\nfk_here:\n\ts_mov_b32 %1, fk_end_c-fk_here\n\ts_mov_b32 %2, fk_end_x-fk_here" : "=s"(pc), "=s"(n1), "=s"(n2));
                n0 = max((int)n1, (int)n2);
                unsigned o = (unsigned)((l & 15) * 32 + r) * 128;
                dma4((const void*)pc, CODEPF && o < n0 ? o : 0, lds0 + L_DUMP);
            }
            asm volatile("" : "+s"(tag));
            BARRIER();
            WAIT_VM(9);
            STAMP(1);
            u32x4 kvo[4];
            {
                const int q = l >> 4;
                const unsigned ka = sc + SC_K + (8 * q + ((l >> 2) & 3)) * 128 + (l & 3) * 8;
#pragma unroll
                for (int cg = 0; cg < 4; ++cg) {
                    const u32x2 sk = *(LAS u32x2*)(sc + SC_SK + (16 * cg + (l & 15)) * 8);
                    u32x2 k0 = __builtin_bit_cast(u32x2, tr_read(ka + cg * 32)), k1 = __builtin_bit_cast(u32x2, tr_read(ka + cg * 32 + 512)), k2 = __builtin_bit_cast(u32x2, tr_read(ka + cg * 32 + 1024));
                    u32x2 v0 = __builtin_bit_cast(u32x2, tr_read(ka + cg * 32 + SC_V)), v1 = __builtin_bit_cast(u32x2, tr_read(ka + cg * 32 + SC_V + 512));
                    if (rb == 0 && q == 0) k0 = (u32x2){0, 0};
                    const float s0 = bf_lo(sk[0]), s1 = bf_hi(sk[0]), s2 = bf_lo(sk[1]), s3 = bf_hi(sk[1]);
                    const float kk[12] = {0.f, bf_hi(k0[0]), bf_lo(k0[1]), bf_hi(k0[1]), bf_lo(k1[0]), bf_hi(k1[0]), bf_lo(k1[1]), bf_hi(k1[1]), bf_lo(k2[0]), bf_hi(k2[0]), bf_lo(k2[1]), bf_hi(k2[1])};
                    const float vv[8] = {bf_lo(v0[0]), bf_hi(v0[0]), bf_lo(v0[1]), bf_hi(v0[1]), bf_lo(v1[0]), bf_hi(v1[0]), bf_lo(v1[1]), bf_hi(v1[1])};
                    float kv[8];
#pragma unroll
                    for (int i = 0; i < 8; ++i) kv[i] = fmul1(ffma1(kk[i + 4], s0, ffma1(kk[i + 3], s1, ffma1(kk[i + 2], s2, fmul1(kk[i + 1], s3)))), vv[i]);
                    kvo[cg] = (u32x4){pk_bf16(kv[7], kv[6]), pk_bf16(kv[5], kv[4]), pk_bf16(kv[3], kv[2]), pk_bf16(kv[1], kv[0])};
                }
            }
            STAMP(2);
            {
                u32x4 gv = *(LAS u32x4*)(sc + SC_GW + l16);
                float a4[4];
#pragma unroll
                for (int rr = 0; rr < 4; ++rr) {
                    u32x4 xv = *(LAS u32x4*)(sc + SC_X + rr * 1024 + l16);
                    float a = 0.f;
                    for (int i = 0; i < 4; ++i) {
                        unsigned pr = pk_bf16(fmul1(bf_lo(xv[i]), bf_lo(gv[i])), fmul1(bf_hi(xv[i]), bf_hi(gv[i])));
                        a = fadd1(a, fadd1(bf_lo(pr), bf_hi(pr)));
                    }
                    a4[rr] = a;
                }
                auto s02 = __builtin_amdgcn_permlane32_swap(__float_as_uint(a4[0]), __float_as_uint(a4[2]), false, false);
                auto s13 = __builtin_amdgcn_permlane32_swap(__float_as_uint(a4[1]), __float_as_uint(a4[3]), false, false);
                float b0 = fadd1(__uint_as_float(s02[0]), __uint_as_float(s02[1])), b1 = fadd1(__uint_as_float(s13[0]), __uint_as_float(s13[1]));
                auto s01 = __builtin_amdgcn_permlane16_swap(__float_as_uint(b0), __float_as_uint(b1), false, false);
                float a = fadd1(__uint_as_float(s01[0]), __uint_as_float(s01[1]));
                a = dpp_add<0xB1>(a); a = dpp_add<0x4E>(a); a = dpp_add<0x141>(a); a = dpp_add<0x140>(a);
                float z = rbf(a);
                float g = rbf(__builtin_amdgcn_rcpf(1.0f + __builtin_amdgcn_exp2f(-1.4426950408889634f * z)));
                asm volatile("" : "+v"(g), "+v"(kvo[0]), "+v"(kvo[1]), "+v"(kvo[2]), "+v"(kvo[3]) :: "memory");
#pragma unroll
                for (int cg = 0; cg < 4; ++cg)
                    __builtin_amdgcn_raw_buffer_store_b128(kvo[cg], wsr, WS_UR + (64 * c + 16 * cg + (l & 15)) * (S * 2) + (2040 - rb - 8 * (l >> 4)) * 2, 0, 16);
                if ((l & 15) == 0) __builtin_amdgcn_raw_buffer_store_b32(__float_as_uint(g), wsr, WS_G + (grow + (l >> 4)) * 4, 0, 16);
            }
            STAMP(10);
            WAIT_VM(0);
#pragma unroll
            for (int i = 0; i < 4; ++i) asm volatile("" : "+v"(hd[i]), "+v"(hp[i]));
            lflag[FL_ST + aw] = 1;
            wait_word(FL_ST + (aw ^ 1));
            if (aw == 0 && l == 0) flag_store(p.ws, WS_F1 + (c * 32 + rt) * 8, tag);
            STAMP(3);
        }
        const int hoff0 = HROT ? (c * 21) >> 3 : 0;
        auto unit_of = [&](int i) { int t = i + hoff0; t = t >= 21 ? t - 21 : t; return i == 21 ? (xw < 2 ? 84 + xw : xw) : 4 * t + xw; };
        const unsigned wA = lds0 + L_WA + xw * 8192, wB = lds0 + L_WB + xw * 6144;
        auto slot_id = [&](int i) { while (i >= BSTEP + 7) i -= 7; return i < BSTEP + 4 ? (i & 3) : 4 + (i - BSTEP - 4); };
        auto in_B = [&](int i) { return slot_id(i) >= 4; };
        auto slot_off = [&](int i) { return (unsigned)((slot_id(i) & 3) * 2048); };
        const unsigned wdo = (unsigned)((l >> 3) * 1024 + c * 128 + (((l & 7) ^ (((l >> 4) & 1) * 4)) * 16));
        auto wd_issue = [&](int i) {
            dma16x2((const char*)p.wd + (size_t)unit_of(i) * 16384, wdo, wdo + 7168, (in_B(i) ? wB : wA) + slot_off(i));
        };
        unsigned wra[2], wrb[2];
        {
            const int g4 = l >> 4, qq = (l >> 2) & 3, pp = l & 3;
            for (int dh = 0; dh < 2; ++dh) {
                const unsigned o = (8 * (g4 >> 1) + qq) * 128 + ((dh ^ (qq >> 1)) * 64) + (16 * (g4 & 1) + 8 * (pp & 1) + 4 * (pp >> 1)) * 2;
                wra[dh] = wA + o; wrb[dh] = wB + o;
            }
        }
        auto wd_read = [&](int i, int dh) {
            unsigned a = (in_B(i) ? wrb[dh] : wra[dh]) + slot_off(i);
            return cat8(tr_read(a), tr_read(a + 512));
        };
        bf16x8 wf[NWR][2];
        if (aux) {
            wait_min4(FL_FREE, WD_START);
            STAMP(4);
            if (aw == 0) { poll_flags(p.ws, (unsigned)(WS_F1 + (l < 32 ? c * 32 + l : (l & 7) * 32 + rt) * 8), tag); lflag[FL_F1] = 1; }
            STAMP(5);
        }
#pragma unroll
        for (int i = 0; i < 4; ++i) wd_issue(i);
        if (!aux) { WAIT_VM(8); lflag[FL_FULL + xw] = 8; STAMP(8); }
#pragma unroll
        for (int i = 0; i < NWR; ++i) {
            if (i == BSTEP) {
                wait_min4(FL_FREE, 6);
                if (aux) {
                    wait_word(FL_F1);
                    const unsigned udst = lds0 + L_UC + aw * UC_BYTES;
                    const char* ub = p.ws + WS_UR + (size_t)(ch0 + aw) * (S * 2);
                    dma16x4c(ub, l16, l16, l16, l16, udst);
                    dma16x4c(ub + 8, l16, l16, l16, l16, udst + UC_STRIDE);
                    if (l < 16) *(LAS u32x4*)(udst + (l >> 3) * UC_STRIDE + 4096 + (l & 7) * 16) = (u32x4){0, 0, 0, 0};
                }
                wd_issue(BSTEP + 4); wd_issue(BSTEP + 5); wd_issue(BSTEP + 6);
            }
            if (i < BSTEP) WAIT_VM(6); else WAIT_VM(12);
            if (i == BSTEP + 4 && aux) {
                if (l == 0) *(LAS u32x2*)(lds0 + L_UC + aw * UC_BYTES + UC_STRIDE + 4088) = (u32x2){0, 0};
                wait_min4(FL_FREE, 7);
            const unsigned hdst = lds0 + L_HC + aw * HC_BYTES;
#pragma unroll
            for (int i = 0; i < 4; ++i) {
                const u32x4 d = hd[i];
                const unsigned e0 = __builtin_amdgcn_alignbit(hp[i][1], hp[i][0], 16), e1 = __builtin_amdgcn_alignbit(d[0], hp[i][1], 16),
                               e2 = __builtin_amdgcn_alignbit(d[1], d[0], 16), e3 = __builtin_amdgcn_alignbit(d[2], d[1], 16), e4 = __builtin_amdgcn_alignbit(d[3], d[2], 16);
                const unsigned a = hdst + 384 + i * 1024 + l16;
                *(LAS u32x4*)(a) = (u32x4){e0, e1, e2, e3};
                *(LAS u32x4*)(a + HC_STRIDE) = (u32x4){hp[i][1], d[0], d[1], d[2]};
                *(LAS u32x4*)(a + 2 * HC_STRIDE) = (u32x4){e1, e2, e3, e4};
                *(LAS u32x4*)(a + 3 * HC_STRIDE) = d;
            }
            if (l < 48) {
                const unsigned a = hdst + (l >= 24 ? HC_STRIDE + (l - 24) * 16 : l16);
                *(LAS u32x4*)(a) = (u32x4){0, 0, 0, 0};
                *(LAS u32x4*)(a + 2 * HC_STRIDE) = (u32x4){0, 0, 0, 0};
            }
                asm volatile("" ::: "memory");
                lflag[FL_UOK + aw] = 1;
                STAMP(9);
            }
            wf[i][0] = wd_read(i, 0); wf[i][1] = wd_read(i, 1);
            unsigned pz = lflag[FL_PAUSE];
            asm volatile("s_waitcnt lgkmcnt(0)" : "+v"(wf[i][0]), "+v"(wf[i][1]), "+v"(pz) :: "memory");
            if (PAUSE && i >= BSTEP + 4 && __builtin_amdgcn_readfirstlane(pz)) wait_min4(FL_FREE, 9);
            wd_issue(i < BSTEP ? i + 4 : i + 7);
        }
        STAMP(6);
        __builtin_amdgcn_s_setprio(0);
        STAMP(11);
        if (xw == 0) {
            wait_min4(FL_FREE, 9);
            poll_flags(p.ws, WS_F2 + (rt * 32 + (l & 31)) * 8, tag);
            lflag[FL_F2] = 1;
        } else {
            WAIT_VM(0);
            if (b == 0 && xw == 1) {
#pragma unroll 1
                for (int it = 0; it < SPIN_CAP; ++it) {
                    u64 v0, v1, v2, v3;
                    asm volatile("global_load_dwordx2 %0, %4, %5 sc1\n\tglobal_load_dwordx2 %1, %4, %5 offset:512 sc1\n\t"
                                 "global_load_dwordx2 %2, %4, %5 offset:1024 sc1\n\tglobal_load_dwordx2 %3, %4, %5 offset:1536 sc1\n\ts_waitcnt vmcnt(0)"
                                 : "=&v"(v0), "=&v"(v1), "=&v"(v2), "=&v"(v3) : "v"(WS_F1 + l * 8), "s"(p.ws) : "memory");
                    if (__all(v0 == tag && v1 == tag && v2 == tag && v3 == tag)) break;
                    __builtin_amdgcn_s_sleep(2);
                }
                if (l == 0) flag_store(p.ws, WS_EP, tag);
            }
            wait_word(FL_F2);
        }
        STAMP(12);
        f32x16 acc[2][2];
        for (int i = 0; i < 16; ++i) { acc[0][0][i] = 0.f; acc[0][1][i] = 0.f; acc[1][0][i] = 0.f; acc[1][1][i] = 0.f; }
        const unsigned hfo = WS_HF + 2 * rt * NUNIT * 1024 + l16;
        bf16x8 hq[22][2];
        auto h_load = [&](int i) {
            const int u = unit_of(i);
            hq[i][0] = __builtin_bit_cast(bf16x8, __builtin_amdgcn_raw_buffer_load_b128(wsr, hfo, u * 1024, 16));
            hq[i][1] = __builtin_bit_cast(bf16x8, __builtin_amdgcn_raw_buffer_load_b128(wsr, hfo + NUNIT * 1024, u * 1024, 16));
        };
#pragma unroll
        for (int i = 0; i < HAHEAD; ++i) { h_load(i); SB(); }
        bf16x8 wt[2][2];
#pragma unroll
        for (int i = 0; i < 22; ++i) {
            if (i + HAHEAD < 22) h_load(i + HAHEAD);
            if (i + 1 >= NWR && i + 1 < 22) { wt[(i + 1) & 1][0] = wd_read(i + 1, 0); wt[(i + 1) & 1][1] = wd_read(i + 1, 1); }
            if (i == F3_AT) lflag[FL_PROG] = 1;
            SB();
            if (i < 21 || xw < 2) {
#pragma unroll
                for (int dh = 0; dh < 2; ++dh) {
                    const bf16x8 wv = i < NWR ? wf[i < NWR ? i : 0][dh] : wt[i & 1][dh];
                    acc[0][dh] = __builtin_amdgcn_mfma_f32_32x32x16_bf16(wv, hq[i][0], acc[0][dh], 0, 0, 0);
                    acc[1][dh] = __builtin_amdgcn_mfma_f32_32x32x16_bf16(wv, hq[i][1], acc[1][dh], 0, 0, 0);
                }
            }
            SB();
        }
        STAMP(19);
#pragma unroll
        for (int t = 0; t < 4; ++t) {
            if (t != xw) {
                const int j = (t - xw - 1) & 3;
                const unsigned dst = (j == 2 ? wB : wA + j * 4096) + l16;
#pragma unroll
                for (int a = 0; a < 4; ++a)
                    *(LAS f32x4*)(dst + a * 1024) = (f32x4){acc[t >> 1][t & 1][4 * a], acc[t >> 1][t & 1][4 * a + 1], acc[t >> 1][t & 1][4 * a + 2], acc[t >> 1][t & 1][4 * a + 3]};
            }
        }
        f32x16 own;
        if (xw == 0) own = acc[0][0]; else if (xw == 1) own = acc[0][1]; else if (xw == 2) own = acc[1][0]; else own = acc[1][1];
        STAMP(21);
        BARRIER();
        STAMP(22);
#pragma unroll
        for (int j = 0; j < 3; ++j) {
            const int v = (xw - 1 - j) & 3;
            const unsigned src = (j == 2 ? lds0 + L_WB + v * 6144 : lds0 + L_WA + v * 8192 + j * 4096) + l16;
#pragma unroll
            for (int a = 0; a < 4; ++a) {
                f32x4 t = *(LAS f32x4*)(src + a * 1024);
                own[4 * a] += t[0]; own[4 * a + 1] += t[1]; own[4 * a + 2] += t[2]; own[4 * a + 3] += t[3];
            }
        }
        {
            const int sh = xw >> 1, dh = xw & 1, s = 32 * sh + (l & 31), hi = l >> 5;
            const float omg = *(LAS float*)(lds0 + L_OMG + s * 4);
#pragma unroll
            for (int r8 = 0; r8 < 2; ++r8) {
                const int cw = 2 * dh + r8;
                u32x4 ga = *(LAS u32x4*)(lds0 + L_RED + (cw ^ 1) * 4096 + 2048 + hi * 1024 + s * 16);
                unsigned o[4];
#pragma unroll
                for (int i = 0; i < 4; ++i) {
                    unsigned bv = pk_bf16(own[8 * r8 + 2 * i], own[8 * r8 + 2 * i + 1]);
                    unsigned t2 = pk_bf16(omg * bf_lo(bv), omg * bf_hi(bv));
                    o[i] = pk_bf16(bf_lo(ga[i]) + bf_lo(t2), bf_hi(ga[i]) + bf_hi(t2));
                }
                *(u32x4*)(p.out + (size_t)(64 * rt + s) * D + 64 * c + 32 * dh + 16 * r8 + 8 * hi) = (u32x4){o[0], o[1], o[2], o[3]};
            }
        }
        asm volatile("fk_end_x:");
    }
}

extern "C" void kernel_launch(void* const* d_in, const int* in_sizes, int n_in,
                              void* d_out, int out_size, void* d_ws, size_t ws_size,
                              hipStream_t stream) {
    Params p;
    p.q = (const u16*)d_in[0]; p.k = (const u16*)d_in[1]; p.v = (const u16*)d_in[2];
    p.shift_k = (const u16*)d_in[3]; p.ssm_k = (const u16*)d_in[4]; p.ssm_D = (const u16*)d_in[5];
    p.wg = (const u16*)d_in[7]; p.wu = (const u16*)d_in[8]; p.wd = (const u16*)d_in[9]; p.gw = (const u16*)d_in[10];
    p.out = (u16*)d_out; p.ws = (char*)d_ws;
    (void)hipFuncSetAttribute((const void*)fused_kernel, hipFuncAttributeMaxDynamicSharedMemorySize, LDS_BYTES);
    fused_kernel<<<256, 512, LDS_BYTES, stream>>>(p);
}
```

```cpp
#include <hip/hip_runtime.h>
#include <hip/hip_bf16.h>
#include <stdint.h>

typedef unsigned short u16;
typedef unsigned long long u64;
typedef short s16x4 __attribute__((ext_vector_type(4)));
typedef __bf16 bf16x8 __attribute__((ext_vector_type(8)));
typedef __bf16 bf16x2 __attribute__((ext_vector_type(2)));
typedef float f32x2 __attribute__((ext_vector_type(2)));
typedef float f32x4 __attribute__((ext_vector_type(4)));
typedef float f32x16 __attribute__((ext_vector_type(16)));
typedef unsigned u32x2 __attribute__((ext_vector_type(2)));
typedef unsigned u32x4 __attribute__((ext_vector_type(4)));
#define LAS __attribute__((address_space(3)))
#define GAS __attribute__((address_space(1)))
#define CAS __attribute__((address_space(4)))

constexpr int S = 2048, D = 512, F = 1376;
constexpr int NUNIT = F / 16;

constexpr unsigned WS_EP = 0;
constexpr unsigned WS_F1 = 4096;
constexpr unsigned WS_F2 = WS_F1 + 4096;
constexpr unsigned WS_F3 = WS_F2 + 8192;
constexpr unsigned WS_G  = 32768;
constexpr unsigned WS_UR = 65536;
constexpr unsigned WS_HF = WS_UR + D * S * 2;
constexpr unsigned WS_Y2 = WS_HF + 64 * NUNIT * 1024;
constexpr unsigned WS_END = WS_Y2 + D * S * 2;

#define NTS " nt"
#define NTA 2
#define NTLOAD(p) __builtin_nontemporal_load(p)
#define HFLAG_AT 24
#define ROT 5
#define HROT 0
#define HAHEAD 6
#define PRIO_C 2
#define PRIO_A 3
#define F3_AT 10
#define CODEPF 1
#define SLP 1
#define PUBAT 16
#define WD_START 5
#define BSTEP 2
#define PAUSE 0
#define AUXFIRST 1
#define THR 12
#define PSLP 8
#define F1_AT 6
#define OUTAUX 0
#define TOUCH 0
#define TLBT 0
#define TAILOV 0
#define WD2_AT 9
#define WD3 22
#define PRIO_L 3
#define WDT 1
#define ROLL 0
#define NTW ""
#define NTX ""
#define WDT2 0
#define HW_AT 2
#define LOOKA 1
#define THR4 8
#define T1EARLY 1
#define F1PUB 3
#define NARROW 1
constexpr unsigned G1_STAGE = 41024, G1_WG = 16384, G1_WU = 16384 + 12288 + 32;
constexpr unsigned HC_STRIDE = 282 * 16, HC_BYTES = 4 * HC_STRIDE;
constexpr unsigned UC_STRIDE = 264 * 16, UC_BYTES = 2 * UC_STRIDE;
constexpr unsigned L_WA = 0;
constexpr unsigned L_RED = G1_STAGE;
constexpr unsigned L_WD = L_RED + 16384;
constexpr unsigned L_UC = 2 * G1_STAGE;
constexpr unsigned L_WB = L_UC + 2 * UC_BYTES;
constexpr unsigned L_HC = L_WB + 4 * 6144;
constexpr unsigned L_SCR = 2 * G1_STAGE;
constexpr unsigned L_DUMP = 161792, L_OMG = 162048;
constexpr unsigned L_FLAG = 163840 - 128;
constexpr unsigned LDS_BYTES = 163840;
constexpr int FL_FULL = 0, FL_UOK = 20, FL_FREE = 4, FL_EXCH = 8, FL_ST = 16, FL_F2 = 22, FL_F3 = 23, FL_PROG = 24, FL_F1 = 26, FL_OMG = 27, FL_PART = 28, FL_GA = 12, FL_CNT = 25;
constexpr int NWR = 11;

__device__ __forceinline__ float bf_lo(unsigned x) { return __uint_as_float(x << 16); }
__device__ __forceinline__ float bf_hi(unsigned x) { return __uint_as_float(x & 0xffff0000u); }
__device__ __forceinline__ unsigned pk_bf16(float a, float b) {
    f32x2 t = {a, b}; bf16x2 p = __builtin_convertvector(t, bf16x2); return __builtin_bit_cast(unsigned, p);
}
__device__ __forceinline__ float rbf(float a) { return bf_lo(pk_bf16(a, a)); }

__device__ __forceinline__ void dma16(const void* sbase, unsigned voff, unsigned lds) {
    asm volatile("s_mov_b32 m0, %2\n\ts_nop 0\n\tglobal_load_lds_dwordx4 %0, %1" :: "v"(voff), "s"(sbase), "s"(lds) : "memory");
}
__device__ __forceinline__ void dma16n(const void* sbase, unsigned voff, unsigned lds) {
    asm volatile("s_mov_b32 m0, %2\n\ts_nop 0\n\tglobal_load_lds_dwordx4 %0, %1" NTS :: "v"(voff), "s"(sbase), "s"(lds) : "memory");
}
__device__ __forceinline__ void dma4n(const void* sbase, unsigned voff, unsigned lds) {
    asm volatile("s_mov_b32 m0, %2\n\ts_nop 0\n\tglobal_load_lds_dword %0, %1" NTS :: "v"(voff), "s"(sbase), "s"(lds) : "memory");
}
__device__ __forceinline__ float fmul1(float a, float b) { float r; asm("v_mul_f32 %0, %1, %2" : "=v"(r) : "v"(a), "v"(b)); return r; }
__device__ __forceinline__ float ffma1(float a, float b, float c) { float r; asm("v_fma_f32 %0, %1, %2, %3" : "=v"(r) : "v"(a), "v"(b), "v"(c)); return r; }
template <int CTRL> __device__ __forceinline__ float dpp_add(float a) {
    return a + __builtin_bit_cast(float, __builtin_amdgcn_update_dpp(0, __builtin_bit_cast(int, a), CTRL, 0xf, 0xf, true));
}
__device__ __forceinline__ float fadd1(float a, float b) { float r; asm("v_add_f32 %0, %1, %2" : "=v"(r) : "v"(a), "v"(b)); return r; }
__device__ __forceinline__ void dma16c(const void* sbase, unsigned voff, unsigned lds) {
    asm volatile("s_mov_b32 m0, %2\n\ts_nop 0\n\tglobal_load_lds_dwordx4 %0, %1 sc1" :: "v"(voff), "s"(sbase), "s"(lds) : "memory");
}
__device__ __forceinline__ void dma4(const void* sbase, unsigned voff, unsigned lds) {
    asm volatile("s_mov_b32 m0, %2\n\ts_nop 0\n\tglobal_load_lds_dword %0, %1" :: "v"(voff), "s"(sbase), "s"(lds) : "memory");
}
#define DMA_G8(SC) \
    "s_mov_b32 m0, %9\n\ts_nop 0\n\t" \
    "global_load_lds_dwordx4 %0, %8 offset:-4096" SC "\n\tglobal_load_lds_dwordx4 %1, %8 offset:-3072" SC "\n\t" \
    "global_load_lds_dwordx4 %2, %8 offset:-2048" SC "\n\tglobal_load_lds_dwordx4 %3, %8 offset:-1024" SC "\n\t" \
    "global_load_lds_dwordx4 %4, %8" SC "\n\tglobal_load_lds_dwordx4 %5, %8 offset:1024" SC "\n\t" \
    "global_load_lds_dwordx4 %6, %8 offset:2048" SC "\n\tglobal_load_lds_dwordx4 %7, %8 offset:3072" SC
__device__ __forceinline__ void dma16x8(const void* sbase, const unsigned* v, unsigned lds) {
    asm volatile(DMA_G8("") :: "v"(v[0]), "v"(v[1]), "v"(v[2]), "v"(v[3]), "v"(v[4]), "v"(v[5]), "v"(v[6]), "v"(v[7]), "s"(sbase), "s"(lds + 4096) : "memory");
}
#define DMA_G4(SC) \
    "s_mov_b32 m0, %5\n\ts_nop 0\n\t" \
    "global_load_lds_dwordx4 %0, %4" SC "\n\tglobal_load_lds_dwordx4 %1, %4 offset:1024" SC "\n\t" \
    "global_load_lds_dwordx4 %2, %4 offset:2048" SC "\n\tglobal_load_lds_dwordx4 %3, %4 offset:3072" SC
__device__ __forceinline__ void dma16x4(const void* sbase, unsigned v0, unsigned v1, unsigned v2, unsigned v3, unsigned lds) {
    asm volatile(DMA_G4("") :: "v"(v0), "v"(v1), "v"(v2), "v"(v3), "s"(sbase), "s"(lds) : "memory");
}
__device__ __forceinline__ void dma16x4w(const void* sbase, unsigned v0, unsigned v1, unsigned v2, unsigned v3, unsigned lds) {
    asm volatile(DMA_G4(NTW) :: "v"(v0), "v"(v1), "v"(v2), "v"(v3), "s"(sbase), "s"(lds) : "memory");
}
__device__ __forceinline__ void dma16x4x(const void* sbase, unsigned v0, unsigned v1, unsigned v2, unsigned v3, unsigned lds) {
    asm volatile(DMA_G4(NTX) :: "v"(v0), "v"(v1), "v"(v2), "v"(v3), "s"(sbase), "s"(lds) : "memory");
}
__device__ __forceinline__ void dma16x4n(const void* sbase, unsigned v0, unsigned v1, unsigned v2, unsigned v3, unsigned lds) {
    asm volatile(DMA_G4(NTS) :: "v"(v0), "v"(v1), "v"(v2), "v"(v3), "s"(sbase), "s"(lds) : "memory");
}
__device__ __forceinline__ void dma16x4c(const void* sbase, unsigned v0, unsigned v1, unsigned v2, unsigned v3, unsigned lds) {
    asm volatile(DMA_G4(" sc1") :: "v"(v0), "v"(v1), "v"(v2), "v"(v3), "s"(sbase), "s"(lds) : "memory");
}
__device__ __forceinline__ void dma16x2(const void* sbase, unsigned v0, unsigned v1, unsigned lds) {
    asm volatile("s_mov_b32 m0, %3\n\ts_nop 0\n\tglobal_load_lds_dwordx4 %0, %2\n\tglobal_load_lds_dwordx4 %1, %2 offset:1024" :: "v"(v0), "v"(v1), "s"(sbase), "s"(lds) : "memory");
}
__device__ __forceinline__ void dma16x2n(const void* sbase, unsigned v0, unsigned v1, unsigned lds) {
    asm volatile("s_mov_b32 m0, %3\n\ts_nop 0\n\tglobal_load_lds_dwordx4 %0, %2" NTS "\n\tglobal_load_lds_dwordx4 %1, %2 offset:1024" NTS :: "v"(v0), "v"(v1), "s"(sbase), "s"(lds) : "memory");
}
__device__ __forceinline__ void dma16x2c(const void* sbase, unsigned v0, unsigned v1, unsigned lds) {
    asm volatile("s_mov_b32 m0, %3\n\ts_nop 0\n\tglobal_load_lds_dwordx4 %0, %2 sc1\n\tglobal_load_lds_dwordx4 %1, %2 offset:1024 sc1" :: "v"(v0), "v"(v1), "s"(sbase), "s"(lds) : "memory");
}
#define WAIT_VM(n) asm volatile("s_waitcnt vmcnt(" #n ")" ::: "memory")
#define WAITN(n) asm volatile("s_waitcnt vmcnt(%0)" :: "i"(n) : "memory")
#define WAIT_LGKM0() asm volatile("s_waitcnt lgkmcnt(0)" ::: "memory")
#define BARRIER() asm volatile("s_waitcnt lgkmcnt(0)\n\ts_barrier" ::: "memory")
#define SB() __builtin_amdgcn_sched_barrier(0)

__device__ __forceinline__ s16x4 tr_read(unsigned addr) { return __builtin_amdgcn_ds_read_tr16_b64_v4i16((LAS s16x4*)addr); }
__device__ __forceinline__ bf16x8 cat8(s16x4 a, s16x4 b) {
    return __builtin_bit_cast(bf16x8, __builtin_shufflevector(a, b, 0, 1, 2, 3, 4, 5, 6, 7));
}
__device__ __forceinline__ bf16x8 lds_read128(unsigned addr) { return *(LAS bf16x8*)addr; }
__device__ __forceinline__ float s2f(short x) { return __uint_as_float((unsigned)(u16)x << 16); }

__device__ __forceinline__ unsigned gelu_mul2(f32x2 g, f32x2 u) {
    const float c1 = -2.0f * 0.7978845608028654f * 1.4426950408889634f;
    const float c2 = c1 * 0.044715f;
    f32x2 t = g * g;
    t = __builtin_elementwise_fma(t, (f32x2){c2, c2}, (f32x2){c1, c1});
    t = t * g;
    f32x2 e = {__builtin_amdgcn_exp2f(t[0]), __builtin_amdgcn_exp2f(t[1])};
    e = e + (f32x2){1.0f, 1.0f};
    f32x2 r = {__builtin_amdgcn_rcpf(e[0]), __builtin_amdgcn_rcpf(e[1])};
    f32x2 h = (g * u) * r;
    return __builtin_bit_cast(unsigned, __builtin_convertvector(h, bf16x2));
}

#define STAMP_DECL
#define ACC_BEGIN()
#define ACC_END(i)
#define STAMP(i)
#define STAMP_FLUSH(kid)

struct Params {
    const u16 *q, *k, *v, *shift_k, *ssm_k, *ssm_D, *wg, *wu, *wd, *gw;
    u16* out;
    char* ws;
};

#define SPIN_CAP (1 << 18)
__device__ __forceinline__ void poll_flags(const char* ws, unsigned voff, u64 tag) {
#pragma unroll 1
    for (int it = 0; it < SPIN_CAP; ++it) {
        u64 v;
        asm volatile("global_load_dwordx2 %0, %1, %2 sc1\n\ts_waitcnt vmcnt(0)" : "=&v"(v) : "v"(voff), "s"(ws) : "memory");
        if (__all(v == tag)) break;
        __builtin_amdgcn_s_sleep(PSLP);
    }
}
__device__ __forceinline__ void flag_store(char* ws, unsigned off, u64 tag) {
    __hip_atomic_store((GAS u64*)(ws + off), tag, __ATOMIC_RELAXED, __HIP_MEMORY_SCOPE_AGENT);
}
static_assert(THR == 12, "");
template <int N> struct IC { static constexpr int v = N; };

__global__ __launch_bounds__(512, 2) void fused_kernel(Params p) {
    extern __shared__ __attribute__((aligned(16))) char smem[];
    const unsigned lds0 = (unsigned)(size_t)(LAS char*)smem;
    const int b = blockIdx.x, xcd = b & 7, r = b >> 3;
    const int tid = threadIdx.x, l = tid & 63;
    const int w = __builtin_amdgcn_readfirstlane(tid >> 6);
    {
        u64 pc; unsigned n1, n2;
        asm volatile("s_getpc_b64 %0\nfk_here:\n\ts_mov_b32 %1, fk_end_c-fk_here\n\ts_mov_b32 %2, fk_end_x-fk_here" : "=s"(pc), "=s"(n1), "=s"(n2));
        const unsigned o = (unsigned)((l & 15) * 32 + r) * 128;
        if (w == 0) dma4((const void*)pc, o < max(n1, n2) ? o : 0, lds0 + L_DUMP);
    }
    const int rt = 4 * xcd + (r >> 3), c = r & 7;
    const int mt = 8 * (xcd >> 2) + (r >> 2), cj = xcd & 3, ci = r & 3, ct = 4 * cj + ci;
    const int st32 = 11 * cj + 3 * ci - (ct == 15);
    const bool narrow = ci == 3 || ct == 14;
    const int ch0 = 64 * c + 2 * rt;
    const int rot = ROT == 1 ? (r >> 2) + 2 * (r & 3) : ROT == 5 ? 4 * (((r >> 2) + (r & 3)) & 1) + 2 * (xcd >> 2) + (xcd & 3) : 0;
    u32x2 sk[2]; u32x4 gv, xv[2], hd[2];
    if (w < 4) {
        __builtin_amdgcn_s_setprio(PRIO_C);
        const unsigned vo = (unsigned)((64 * rt + 16 * w + (l >> 3)) * 1024 + c * 128 + (l & 7) * 16);
        const unsigned sc = lds0 + L_SCR + 16384 + w * 5120;
        dma16x2n(p.k, rt + w == 0 && l < 32 ? vo : vo - 4096, vo - 4096 + 7168, sc);
        dma16n(p.k, vo + (12 - ((l >> 3) & 4)) * 1024, sc + 2048);
        dma16x2n(p.v, vo, vo + 7168, sc + 3072);
#pragma unroll
        for (int ps = 0; ps < 2; ++ps) sk[ps] = *(const u32x2*)((const char*)p.shift_k + (unsigned)((64 * c + 32 * ps + 16 * (l >> 5) + (l & 15)) * 8));
        gv = *(const u32x4*)((const char*)p.gw + l * 16);
#pragma unroll
        for (int rr = 0; rr < 2; ++rr) xv[rr] = *(const u32x4*)((const char*)p.q + (unsigned)((64 * rt + 8 * c + 2 * w + rr) * 1024 + l * 16));
#pragma unroll
        for (int i = 0; i < 2; ++i) hd[i] = NTLOAD((const u32x4*)((const char*)p.ssm_k + (unsigned)(ch0 * (S * 2) + w * 2048 + i * 1024 + l * 16)));
        asm volatile("" ::: "memory");
    }
    u64 tag;
#define LOAD_TAG() do { asm volatile("" : "+s"(tag)); tag += 1; } while (0)
#define wsr __builtin_amdgcn_make_buffer_rsrc(p.ws, 0, WS_END, 0x00020000)
    const int xw = w & 3;
    const unsigned l16 = l * 16;
    const unsigned scr = lds0 + L_SCR;
    volatile LAS unsigned* lflag = (volatile LAS unsigned*)(lds0 + L_FLAG);
    auto wait_min4 = [&](int idx, unsigned n) {
#pragma unroll 1
        for (int it = 0; it < SPIN_CAP; ++it) {
            u32x4 f = *(volatile LAS u32x4*)(lds0 + L_FLAG + idx * 4);
            if (__builtin_amdgcn_readfirstlane(min(min(f[0], f[1]), min(f[2], f[3]))) >= n) break;
            __builtin_amdgcn_s_sleep(SLP);
        }
        asm volatile("" ::: "memory");
    };
    auto wait_word = [&](int idx) {
#pragma unroll 1
        for (int it = 0; it < SPIN_CAP; ++it) { if (__builtin_amdgcn_readfirstlane(lflag[idx])) break; __builtin_amdgcn_s_sleep(1); }
        asm volatile("" ::: "memory");
    };

    if (w < 4) {
        const int wm = w & 1, wn = w >> 1;
        const int nj = (NARROW && narrow) ? 2 : 3;
        unsigned xa[2][4], wa[2];
        unsigned dpair;
        {
            const int q = l >> 4;
            tag = *(volatile const CAS u64*)(p.ws + WS_EP);
            dpair = *(volatile const CAS unsigned*)(p.ssm_D + ch0);
            u32x2 hb = *(volatile const CAS u32x2*)(p.ssm_k + (size_t)(ch0 + (w >> 1)) * S + 1020);
            if (w == 0 && l < 32) lflag[l] = 0;
            {
                const int sr = l & 31, hi = l >> 5;
                for (int ks = 0; ks < 4; ++ks) { xa[0][ks] = lds0 + (64 * wm + sr) * 128 + 16 * ((2 * ks + hi) ^ ((sr >> 1) & 7)); xa[1][ks] = xa[0][ks] + 2 * G1_STAGE; }
                const int qq = (l >> 2) & 3, pp = l & 3;
                wa[0] = lds0 + ((pp >> 1) ? G1_WU : G1_WG) + (8 * (q >> 1) + qq) * 192 + (nj * wn) * 32 + (2 * (pp & 1) + (q & 1)) * 8;
                wa[1] = wa[0] + 2 * G1_STAGE;
                for (int ks = 0; ks < 4; ++ks) asm volatile("" : "+v"(xa[0][ks]), "+v"(xa[1][ks]));
                asm volatile("" : "+v"(wa[0]), "+v"(wa[1]));
            }
            const unsigned hdst = lds0 + L_HC + (w >> 1) * HC_BYTES;
            if (l < 48) *(LAS u32x4*)(hdst + (w & 1) * 2 * HC_STRIDE + (l >= 24 ? HC_STRIDE + (l - 24) * 16 : l16)) = (u32x4){0, 0, 0, 0};
            WAIT_VM(7);
            STAMP(2);
#pragma unroll
            for (int ps = 0; ps < 2; ++ps) {
                const unsigned ka = scr + 16384 + w * 5120 + (8 * (q & 1) + ((l >> 2) & 3)) * 128 + (2 * ps + (q >> 1)) * 32 + (l & 3) * 8;
                u32x2 k0 = __builtin_bit_cast(u32x2, tr_read(ka)), k1 = __builtin_bit_cast(u32x2, tr_read(ka + 512)), k2 = __builtin_bit_cast(u32x2, tr_read(ka + 1024));
                u32x2 v0 = __builtin_bit_cast(u32x2, tr_read(ka + 3072)), v1 = __builtin_bit_cast(u32x2, tr_read(ka + 3072 + 512));
                if (rt + w == 0 && !(q & 1)) k0 = (u32x2){0, 0};
                const float s0 = bf_lo(sk[ps][0]), s1 = bf_hi(sk[ps][0]), s2 = bf_lo(sk[ps][1]), s3 = bf_hi(sk[ps][1]);
                const float kk[12] = {0.f, bf_hi(k0[0]), bf_lo(k0[1]), bf_hi(k0[1]), bf_lo(k1[0]), bf_hi(k1[0]), bf_lo(k1[1]), bf_hi(k1[1]), bf_lo(k2[0]), bf_hi(k2[0]), bf_lo(k2[1]), bf_hi(k2[1])};
                const float vv[8] = {bf_lo(v0[0]), bf_hi(v0[0]), bf_lo(v0[1]), bf_hi(v0[1]), bf_lo(v1[0]), bf_hi(v1[0]), bf_lo(v1[1]), bf_hi(v1[1])};
                float kv[8];
#pragma unroll
                for (int i = 0; i < 8; ++i) kv[i] = (kk[i + 1] * s3 + kk[i + 2] * s2 + kk[i + 3] * s1 + kk[i + 4] * s0) * vv[i];
                *(LAS u32x4*)(scr + (2 * ps + (q >> 1)) * 4096 + (l & 15) * 128 + 16 * ((7 - 2 * w - (q & 1)) ^ (l & 7))) =
                    (u32x4){pk_bf16(kv[7], kv[6]), pk_bf16(kv[5], kv[4]), pk_bf16(kv[3], kv[2]), pk_bf16(kv[1], kv[0])};
            }
            {
                float a2[2];
#pragma unroll
                for (int rr = 0; rr < 2; ++rr) {
                    float a = 0.f;
                    for (int i = 0; i < 4; ++i) {
                        unsigned pr = pk_bf16(bf_lo(xv[rr][i]) * bf_lo(gv[i]), bf_hi(xv[rr][i]) * bf_hi(gv[i]));
                        a += bf_lo(pr) + bf_hi(pr);
                    }
                    a2[rr] = a;
                }
                auto s01 = __builtin_amdgcn_permlane32_swap(__float_as_uint(a2[0]), __float_as_uint(a2[1]), false, false);
                float a = __uint_as_float(s01[0]) + __uint_as_float(s01[1]);
                auto s16 = __builtin_amdgcn_permlane16_swap(__float_as_uint(a), __float_as_uint(a), false, false);
                a = __uint_as_float(s16[0]) + __uint_as_float(s16[1]);
                a = dpp_add<0xB1>(a); a = dpp_add<0x4E>(a); a = dpp_add<0x141>(a); a = dpp_add<0x140>(a);
                const float z = rbf(a);
                const float g = rbf(__builtin_amdgcn_rcpf(1.0f + __builtin_amdgcn_exp2f(-1.4426950408889634f * z)));
                if ((l & 31) == 0) *(LAS float*)(scr + 2048 + (2 * w + (l >> 5)) * 4) = g;
            }
#pragma unroll
            for (int i = 0; i < 2; ++i) {
                const u32x4 d = hd[i];
                const unsigned b0 = i ? __builtin_amdgcn_readlane(hd[0][2], 63) : (w & 1) ? hb[0] : 0u, b1 = i ? __builtin_amdgcn_readlane(hd[0][3], 63) : (w & 1) ? hb[1] : 0u;
                const unsigned p0 = __builtin_amdgcn_update_dpp(b0, d[2], 0x138, 0xf, 0xf, false), p1 = __builtin_amdgcn_update_dpp(b1, d[3], 0x138, 0xf, 0xf, false);
                const unsigned e0 = __builtin_amdgcn_alignbit(p1, p0, 16), e1 = __builtin_amdgcn_alignbit(d[0], p1, 16),
                               e2 = __builtin_amdgcn_alignbit(d[1], d[0], 16), e3 = __builtin_amdgcn_alignbit(d[2], d[1], 16), e4 = __builtin_amdgcn_alignbit(d[3], d[2], 16);
                const unsigned a = hdst + 384 + ((w & 1) * 2 + i) * 1024 + l16;
                *(LAS u32x4*)(a) = (u32x4){e0, e1, e2, e3};
                *(LAS u32x4*)(a + HC_STRIDE) = (u32x4){p1, d[0], d[1], d[2]};
                *(LAS u32x4*)(a + 2 * HC_STRIDE) = (u32x4){e1, e2, e3, e4};
                *(LAS u32x4*)(a + 3 * HC_STRIDE) = d;
            }
            STAMP(3);
        }
        asm volatile("" : "+s"(dpair));
        LOAD_TAG();
        auto wait_full = [&](unsigned n) {
#pragma unroll 1
            for (int it = 0; it < SPIN_CAP; ++it) {
                u32x4 f = *(volatile LAS u32x4*)(lds0 + L_FLAG + FL_FULL * 4);
                if (__builtin_amdgcn_readfirstlane(min(min(f[0], f[1]), min(f[2], f[3]))) >= n) break;
            }
            asm volatile("" ::: "memory");
        };
        const int chn = w >> 1, chalf = w & 1;
        const unsigned ha = lds0 + L_HC + chn * HC_BYTES + (l & 3) * HC_STRIDE + (8 * ((l & 15) >> 2) + (l >> 4)) * 16 + chalf * 256;
        const unsigned ua = lds0 + L_UC + chn * UC_BYTES + ((l & 1) ^ 1) * UC_STRIDE + (7 - ((l & 15) >> 1) + (l >> 4)) * 16 + chalf * 256;
        bf16x8 uf0[8], hf0[8];
        unsigned cflag;
        auto conv_first = [&]() {
            const u32x4 f = *(volatile LAS u32x4*)(lds0 + L_FLAG + FL_FREE * 4);
            const unsigned uk = lflag[FL_UOK + chn];
            asm volatile("" ::: "memory");
            hf0[0] = lds_read128(ha);
#pragma unroll
            for (int e = 7; e >= 0; --e) uf0[e] = lds_read128(ua + (7 - e) * 512);
#pragma unroll
            for (int a = 1; a < 8; ++a) hf0[a] = lds_read128(ha + a * 512);
            asm volatile("" ::: "memory");
            cflag = uk && min(min(f[0], f[1]), min(f[2], f[3])) >= 8;
        };
        auto gemm1 = [&](auto njc) {
            constexpr int NJ = decltype(njc)::v;
            f32x16 acc[2][NJ];
            for (int s = 0; s < 2; ++s) for (int j = 0; j < NJ; ++j) for (int i = 0; i < 16; ++i) acc[s][j][i] = 0.f;
            constexpr int LA = LOOKA, NB = LA + 1;
            bf16x8 xb[NB][2]; s16x4 al[NB][NJ], ah[NB][NJ];
            BARRIER();
            STAMP(1);
            u32x4 kvo[2];
#pragma unroll
            for (int t = 0; t < 2; ++t) { const int chl = 8 * t + (l >> 3); kvo[t] = *(LAS u32x4*)(scr + w * 4096 + chl * 128 + 16 * ((l & 7) ^ (chl & 7))); }
            const unsigned gq = *(LAS unsigned*)(scr + 2048 + (l & 7) * 4);
            asm volatile("" ::: "memory");
            lflag[FL_ST + w] = 1;
#pragma unroll
            for (int g = 0; g < LA; ++g) {
                xb[g][0] = lds_read128(xa[0][g]); al[g][0] = tr_read(wa[0] + g * 3072); ah[g][0] = tr_read(wa[0] + g * 3072 + 768); xb[g][1] = lds_read128(xa[0][g] + 4096);
                for (int j = 1; j < NJ; ++j) { al[g][j] = tr_read(wa[0] + g * 3072 + j * 32); ah[g][j] = tr_read(wa[0] + g * 3072 + j * 32 + 768); }
            }
#pragma unroll
            for (int t = 0; t < 2; ++t)
                __builtin_amdgcn_raw_buffer_store_b128(kvo[t], wsr, WS_UR + (64 * c + 16 * w + 8 * t + (l >> 3)) * (S * 2) + (1984 - 64 * rt) * 2 + (l & 7) * 16, 0, 16);
            if (w == 0 && l < 8) __builtin_amdgcn_raw_buffer_store_b32(gq, wsr, WS_G + (64 * rt + 8 * c + l) * 4, 0, 16);
            SB();
            u32x4 fl; unsigned fm = 0, fs = 0;
#pragma unroll
            for (int g = 0; g < 32; ++g) {
                const int cur = g % NB, nx = (g + LA) % NB, ng = g + LA, nkt = ng >> 2, nks = ng & 3, st = nkt % 3;
                const bool rd = ng < 32;
                const unsigned xr = xa[st == 2][nks] + (st == 1 ? G1_STAGE : 0), wr = wa[st == 2] + (st == 1 ? G1_STAGE : 0) + nks * 3072;
#pragma unroll
                for (int i = 0; i < 2 * NJ; ++i) {
                    const int j = i >> 1, s = i & 1;
                    acc[s][j] = __builtin_amdgcn_mfma_f32_32x32x16_bf16(cat8(al[cur][j], ah[cur][j]), xb[cur][s], acc[s][j], 0, 0, 0);
                    SB();
                    if (nks == 0 && i == 0 && ng <= 32) {
                        asm volatile("" ::: "memory"); lflag[FL_FREE + w] = nkt;
                        if (nkt == F1PUB) {
                            WAIT_VM(0);
                            if (l == 0) { if (__hip_atomic_fetch_add((LAS unsigned*)(lds0 + L_FLAG + FL_CNT * 4), 1u, __ATOMIC_RELAXED, __HIP_MEMORY_SCOPE_WORKGROUP) == 3) flag_store(p.ws, WS_F1 + (c * 32 + rt) * 8, tag); }
                            SB();
                        }
                        if (rd && __builtin_expect(fs < (unsigned)(nkt + 1), 0)) { ACC_BEGIN(); wait_full(nkt + 1); ACC_END(20); }
                    }
                    if (rd) {
                        if (i == 0) { xb[nx][0] = lds_read128(xr); al[nx][0] = tr_read(wr); }
                        if (i == 1) { ah[nx][0] = tr_read(wr + 768); xb[nx][1] = lds_read128(xr + 4096); }
                        if (i >= 2) { if (i & 1) ah[nx][j] = tr_read(wr + j * 32 + 768); else al[nx][j] = tr_read(wr + j * 32); }
                    }
                    if (ng < 28) {
                        if (nks == 2 && i == 2 * NJ - 1) fl = *(volatile LAS u32x4*)(lds0 + L_FLAG + FL_FULL * 4);
                        if (nks == 3 && i == 2 * NJ - 2) fm = min(min(fl[0], fl[1]), min(fl[2], fl[3]));
                        if (nks == 3 && i == 2 * NJ - 1) fs = __builtin_amdgcn_readfirstlane(fm);
                    }
                    SB();
                }
            }
            STAMP(9);
#pragma unroll
            for (int j = 0; j < NJ; ++j) {
#pragma unroll
                for (int s = 0; s < 2; ++s) {
                    if (j == NJ - 1 && s == 1) conv_first();
                    const f32x16 v = acc[s][j];
                    u32x4 o = {gelu_mul2((f32x2){v[0], v[1]}, (f32x2){v[4], v[5]}), gelu_mul2((f32x2){v[2], v[3]}, (f32x2){v[6], v[7]}),
                               gelu_mul2((f32x2){v[8], v[9]}, (f32x2){v[12], v[13]}), gelu_mul2((f32x2){v[10], v[11]}, (f32x2){v[14], v[15]})};
                    if (NARROW || !narrow || NJ * wn + j < 4)
                        __builtin_amdgcn_raw_buffer_store_b128(o, wsr, WS_HF + ((4 * mt + 2 * wm + s) * NUNIT + 2 * st32 + NJ * wn + j) * 1024 + l16, 0, 16);
                }
            }
        };
        if (NARROW && narrow) gemm1(IC<2>{}); else gemm1(IC<3>{});
        STAMP(10);
        const unsigned f2off = WS_F2 + ((2 * mt + wm) * 32 + wn * 16 + ct) * 8;
        bool hpub = false;
        if (!__builtin_amdgcn_readfirstlane(cflag)) {
            WAIT_VM(0);
            if (l == 0) flag_store(p.ws, f2off, tag);
            hpub = true;
            lflag[FL_FREE + w] = 9;
            wait_word(FL_UOK + chn);
            wait_min4(FL_FREE, 8);
            conv_first();
        }
        STAMP(11);
        const unsigned red = lds0 + L_RED;
        auto conv = [&](auto chc) {
            constexpr int CH = decltype(chc)::v;
            constexpr int KEEP[4] = {CH ? 6 : 7, CH ? 5 : 4, CH ? 2 : 3, CH ? 1 : 0}, SEND[4] = {CH ? 7 : 6, CH ? 4 : 5, CH ? 3 : 2, CH ? 0 : 1};
            f32x4 ca[8];
            const unsigned uo = lds0 + L_UC + chn * UC_BYTES + (252 - 4 * l) * 2;
            for (int n = 0; n < 8; ++n) ca[n] = (f32x4){0, 0, 0, 0};
            bf16x8 uf[2][8], hf[2][8];
#pragma unroll
            for (int i = 0; i < 8; ++i) { uf[0][i] = uf0[i]; hf[0][i] = hf0[i]; }
            SB();
#pragma unroll
            for (int cc = 0; cc < 3; ++cc) {
                const int cur = cc & 1, nx = cur ^ 1;
                int cnt = 0;
#pragma unroll
                for (int a = 0; a < 8; ++a) {
#pragma unroll
                    for (int e = 7 - a; e >= 0; --e) {
                        ca[a + e] = __builtin_amdgcn_mfma_f32_16x16x32_bf16(hf[cur][a], uf[cur][e], ca[a + e], 0, 0, 0);
                        SB();
                        if ((cnt & 1) == 0 && cnt < 32) {
                            const int ri = cnt >> 1;
                            if (ri == 0) hf[nx][0] = lds_read128(ha + (cc + 1) * 64);
                            else if (ri <= 8) uf[nx][8 - ri] = lds_read128(ua + (cc + 1) * 64 + (ri - 1) * 512);
                            else hf[nx][ri - 8] = lds_read128(ha + (cc + 1) * 64 + (ri - 8) * 512);
                            SB();
                        }
                        if (cc * 36 + cnt == HFLAG_AT) {
                            WAIT_VM(0);
                            if (l == 0 && !hpub) flag_store(p.ws, f2off, tag);
                            lflag[FL_FREE + w] = 9;
                            STAMP(12);
                            SB();
                        }
                        ++cnt;
                    }
                }
            }
            u32x2 uu[4];
#pragma unroll
            for (int t = 0; t < 8; ++t) {
                const int n = t < 4 ? SEND[t] : KEEP[t - 4];
#pragma unroll
                for (int a = 0; a <= n; ++a) {
                    ca[n] = __builtin_amdgcn_mfma_f32_16x16x32_bf16(hf[1][a], uf[1][n - a], ca[n], 0, 0, 0);
                    SB();
                    if (t == 0 && a < 4) { uu[a] = *(LAS u32x2*)(uo + (7 - KEEP[a]) * 512); SB(); }
                    if (t >= 1 && t <= 2 && a == 2) { *(LAS f32x4*)(red + w * 4096 + (t - 1) * 1024 + l16) = ca[SEND[t - 1]]; SB(); }
                    if (t == 4 && a == 2) { *(LAS f32x4*)(red + w * 4096 + 2048 + l16) = ca[SEND[2]]; SB(); }
                    if (t == 4 && a == 3) { *(LAS f32x4*)(red + w * 4096 + 3072 + l16) = ca[SEND[3]]; asm volatile("" ::: "memory"); lflag[FL_EXCH + w] = 1; SB(); }
                }
            }
#pragma unroll
            for (int i = 0; i < 4; ++i) asm volatile("" : "+v"(ca[KEEP[i]]));
            STAMP(13);
            const float Dd = chn ? bf_hi(dpair) : bf_lo(dpair);
            f32x4 o[4];
#pragma unroll 1
            for (int it = 0; it < SPIN_CAP; ++it) {
                const unsigned f = lflag[FL_EXCH + (w ^ 1)];
#pragma unroll
                for (int i = 0; i < 4; ++i) o[i] = *(volatile LAS f32x4*)(red + (w ^ 1) * 4096 + i * 1024 + l16);
                if (__builtin_amdgcn_readfirstlane(f)) break;
            }
            u32x2 r2[4];
#pragma unroll
            for (int i = 0; i < 4; ++i) {
                const f32x4 m = ca[KEEP[i]];
                r2[i] = (u32x2){pk_bf16(ffma1(bf_hi(uu[i][1]), Dd, fadd1(o[i][0], m[0])), ffma1(bf_lo(uu[i][1]), Dd, fadd1(o[i][1], m[1]))),
                                pk_bf16(ffma1(bf_hi(uu[i][0]), Dd, fadd1(o[i][2], m[2])), ffma1(bf_lo(uu[i][0]), Dd, fadd1(o[i][3], m[3])))};
                __builtin_amdgcn_raw_buffer_store_b64(r2[i], wsr, WS_Y2 + (ch0 + chn) * (S * 2) + KEEP[i] * 512 + l * 8, 0, 16);
            }
        };
        { if (chalf) conv(IC<1>{}); else conv(IC<0>{}); }
        STAMP(14);
        WAIT_VM(0);
        if (l == 0) flag_store(p.ws, WS_F3 + ((c * 2 + chalf) * 64 + 2 * rt + chn) * 8, tag);
        STAMP(15);
        {
            const int sg = 64 * rt + l, dg = 64 * c + 16 * w;
            u32x4 q0 = *(const u32x4*)(p.q + (size_t)sg * D + dg), q1 = *(const u32x4*)(p.q + (size_t)sg * D + dg + 8);
            float g = __uint_as_float(__builtin_amdgcn_raw_buffer_load_b32(wsr, WS_G + sg * 4, 0, 16));
            if (w == 0) {
                *(LAS float*)(lds0 + L_OMG + l * 4) = rbf(1.0f - g);
                asm volatile("" ::: "memory");
                lflag[FL_OMG] = 1;
                wait_word(FL_PROG);
                poll_flags(p.ws, WS_F3 + ((c * 2 + ((0x66 >> (rt >> 2)) & 1)) * 64 + l) * 8, tag);
                lflag[FL_F3] = 1;
            } else wait_word(FL_F3);
            STAMP(16);
            const unsigned yl = red + (w ^ 1) * 4096;
            const char* ysrc = p.ws + WS_Y2 + (size_t)dg * (S * 2) + rt * 128;
            const unsigned yoff = (unsigned)((l >> 3) * (S * 2) + (l & 7) * 16);
            dma16x2c(ysrc, yoff, yoff + 8 * S * 2 - 1024, yl);
            WAIT_VM(0);
            STAMP(17);
            const unsigned ya = yl + ((l >> 2) & 3) * 128 + (16 * (l >> 4) + 4 * (l & 3)) * 2;
            unsigned ga[8];
#pragma unroll
            for (int i = 0; i < 4; ++i) {
                s16x4 y = tr_read(ya + i * 512);
                unsigned qa = i < 2 ? q0[2 * i] : q1[2 * i - 4], qb = i < 2 ? q0[2 * i + 1] : q1[2 * i - 3];
                unsigned a0 = pk_bf16(fmul1(bf_lo(qa), s2f(y[0])), fmul1(bf_hi(qa), s2f(y[1])));
                unsigned a1 = pk_bf16(fmul1(bf_lo(qb), s2f(y[2])), fmul1(bf_hi(qb), s2f(y[3])));
                ga[2 * i] = pk_bf16(fmul1(g, bf_lo(a0)), fmul1(g, bf_hi(a0)));
                ga[2 * i + 1] = pk_bf16(fmul1(g, bf_lo(a1)), fmul1(g, bf_hi(a1)));
            }
            *(LAS u32x4*)(yl + 2048 + l16) = (u32x4){ga[0], ga[1], ga[2], ga[3]};
            *(LAS u32x4*)(yl + 3072 + l16) = (u32x4){ga[4], ga[5], ga[6], ga[7]};
            asm volatile("" ::: "memory");
            lflag[FL_GA + w] = 2;
        }
        STAMP(21);
        asm volatile("fk_end_c:");
    } else {
        tag = *(volatile const CAS u64*)(p.ws + WS_EP);
        const bool aux = xw >= 2;
        const int aw = xw & 1;
        {
            unsigned xo[4], wo[6];
#pragma unroll
            for (int j = 0; j < 3; ++j) {
                const int n = 64 * j + l, row = (n * 171) >> 11;
                int cc = n - 12 * row;
                if (narrow && cc >= 8) cc -= 4;
                const unsigned o = (unsigned)(row * (F * 2) + cc * 16);
                wo[j] = o - j * 1024; wo[j + 3] = o + 16 * (F * 2) - ((j + 3) & 3) * 1024;
            }
            xo[0] = (unsigned)((l >> 3) * 1024 + (((l & 7) ^ (l >> 4)) * 16));
            xo[1] = (xo[0] ^ 64) + 7168; xo[2] = xo[0] + 2 * 7168; xo[3] = xo[1] + 2 * 7168;
            const char* xsrc = (const char*)p.q + (size_t)(128 * mt + 32 * xw) * 1024;
            const char* wsrc = (const char*)(xw >> 1 ? p.wu : p.wg) + st32 * 64 + (size_t)(32 * (xw & 1)) * (F * 2);
            unsigned so = 0;
#pragma unroll 1
            for (int i = 0; i < 8; ++i) {
                const int kt = (i + rot) & 7;
                const char* xb = xsrc + kt * 128;
                const char* wb = wsrc + (size_t)kt * 64 * (F * 2);
                const unsigned xl = lds0 + so + xw * 4096, wl = lds0 + so + (xw >> 1 ? G1_WU : G1_WG) + (xw & 1) * 6144;
                if (i == 2) {
                    WAIT_VM(10);
                    LOAD_TAG();
                    BARRIER();
                    __builtin_amdgcn_s_setprio(PRIO_L);
                    STAMP(1);
                }
                if (i >= 3) {
                    u32x4 f = *(volatile LAS u32x4*)(lds0 + L_FLAG + FL_FREE * 4);
                    if (__builtin_amdgcn_readfirstlane(min(min(f[0], f[1]), min(f[2], f[3]))) < (unsigned)(i - 2)) {
                        ACC_BEGIN();
                        WAIT_VM(0);
                        lflag[FL_FULL + xw] = i;
                        wait_min4(FL_FREE, i - 2);
                        ACC_END(20);
                    }
                }
                dma16x4w(wb, wo[0], wo[1], wo[2], wo[3], wl);
                WAITN(THR4);
                if (i == 2) wait_word(FL_ST + xw);
                dma16x4x(xb, xo[0], xo[1], xo[2], xo[3], xl);
                WAIT_VM(8);
                if (i >= 2) { lflag[FL_FULL + xw] = i; STAMP(i == 6 ? 13 : i == 7 ? 14 : i); }
                dma16x2(wb, wo[4], wo[5], wl + 4096);
                if (i == 0 && xw == 2) dma4(p.wd, (unsigned)((b * 43 + (l < 43 ? l : 42)) * 128), lds0 + L_DUMP);
                if (i == 0) STAMP(18);
                WAITN(THR4);
                so = so == 2 * G1_STAGE ? 0 : so + G1_STAGE;
            }
            {
                u32x4 f = *(volatile LAS u32x4*)(lds0 + L_FLAG + FL_FREE * 4);
                if (aux || __builtin_amdgcn_readfirstlane(min(min(f[0], f[1]), min(f[2], f[3]))) < 6) { WAIT_VM(0); lflag[FL_FULL + xw] = 8; }
            }
            __builtin_amdgcn_s_setprio(PRIO_A);
        }
        const int hoff0 = HROT ? (c * 21) >> 3 : 0;
        auto unit_of = [&](int i) { int t = i + hoff0; t = t >= 21 ? t - 21 : t; return i == 21 ? (xw < 2 ? 84 + xw : xw) : 4 * t + xw; };
        const unsigned wA = lds0 + L_WA + xw * 10240, wB = lds0 + L_WB + xw * 6144, wD = lds0 + L_WD + xw * 6144;
        auto area = [&](int i) { return i >= 19 ? 2 : (i & 7) < 3 ? 1 : 0; };
        auto soff = [&](int i) { return (unsigned)((i >= 19 ? i - 19 : (i & 7) < 3 ? (i & 7) : (i & 7) - 3) * 2048); };
        const unsigned wdo = (unsigned)((l >> 3) * 1024 + c * 128 + (((l & 7) ^ (((l >> 4) & 1) * 4)) * 16));
        auto wd_issue = [&](int i0, int i1) {
#pragma unroll
            for (int i = i0; i < i1; ++i)
                dma16x2((const char*)p.wd + (size_t)unit_of(i) * 16384, wdo, wdo + 7168, (area(i) == 0 ? wA : area(i) == 1 ? wB : wD) + soff(i));
        };
        unsigned wra[2], wrd[2];
        {
            const int g4 = l >> 4, qq = (l >> 2) & 3, pp = l & 3;
            for (int dh = 0; dh < 2; ++dh) {
                const unsigned o = (8 * (g4 >> 1) + qq) * 128 + ((dh ^ aw ^ (qq >> 1)) * 64) + (16 * (g4 & 1) + 8 * (pp & 1) + 4 * (pp >> 1)) * 2;
                wra[dh] = wA + o; wrd[dh] = wD + o;
            }
        }
        auto wd_read = [&](int i, int dh) {
            unsigned a = (area(i) == 0 ? wra[dh] : area(i) == 1 ? wrd[dh] + (L_WB - L_WD) : wrd[dh]) + soff(i);
            return cat8(tr_read(a), tr_read(a + 512));
        };
        bf16x8 wf[NWR][2];
        auto wd_take = [&](int i0, int i1) {
#pragma unroll
            for (int i = i0; i < i1; ++i) { wf[i][0] = wd_read(i, 0); wf[i][1] = wd_read(i, 1); }
            WAIT_LGKM0();
#pragma unroll
            for (int i = i0; i < i1; ++i) asm volatile("" : "+v"(wf[i][0]), "+v"(wf[i][1]));
        };
        if (aux) {
            wait_min4(FL_FREE, F1_AT);
            STAMP(4);
            if (aw == 0) { poll_flags(p.ws, (unsigned)(WS_F1 + (l < 32 ? c * 32 + l : (l & 7) * 32 + rt) * 8), tag); lflag[FL_F1] = 1; }
            else wait_word(FL_F1);
            STAMP(5);
        }
        wait_min4(FL_FREE, 6);
        if (aux) {
            const unsigned udst = lds0 + L_UC + aw * UC_BYTES;
            const char* ub = p.ws + WS_UR + (size_t)(ch0 + aw) * (S * 2);
            dma16x4c(ub, l16, l16, l16, l16, udst);
            dma16x4c(ub + 8, l16, l16, l16, l16, udst + UC_STRIDE);
            if (l < 16) *(LAS u32x4*)(udst + (l >> 3) * UC_STRIDE + 4096 + (l & 7) * 16) = (u32x4){0, 0, 0, 0};
        }
        wd_issue(0, 3);
        WAIT_VM(6);
        if (aux) {
            if (l == 0) *(LAS u32x2*)(lds0 + L_UC + aw * UC_BYTES + UC_STRIDE + 4088) = (u32x2){0, 0};
            asm volatile("" ::: "memory");
            lflag[FL_UOK + aw] = 1;
            STAMP(9);
        } else { lflag[FL_FULL + xw] = 8; STAMP(8); }
        WAIT_VM(0);
        wd_take(0, 3);
        wd_issue(8, 11);
        STAMP(7);
        wait_min4(FL_FREE, 7);
        wd_issue(3, 8);
        WAIT_VM(10);
        wd_take(8, 11);
        WAIT_VM(0);
        wd_take(3, 8);
        STAMP(17);
        __builtin_amdgcn_s_setprio(0);
        wait_min4(FL_FREE, WD2_AT);
        wd_issue(11, WD3);
        STAMP(6);
        STAMP(11);
        if (xw == 0) {
            poll_flags(p.ws, WS_F2 + (rt * 32 + (l & 31)) * 8, tag);
            lflag[FL_F2] = 1;
        } else {
            if (b == 0 && xw == 1) {
#pragma unroll 1
                for (int it = 0; it < SPIN_CAP; ++it) {
                    u64 v0, v1, v2, v3;
                    asm volatile("global_load_dwordx2 %0, %4, %5 sc1\n\tglobal_load_dwordx2 %1, %4, %5 offset:512 sc1\n\t"
                                 "global_load_dwordx2 %2, %4, %5 offset:1024 sc1\n\tglobal_load_dwordx2 %3, %4, %5 offset:1536 sc1\n\ts_waitcnt vmcnt(0)"
                                 : "=&v"(v0), "=&v"(v1), "=&v"(v2), "=&v"(v3) : "v"(WS_F1 + l * 8), "s"(p.ws) : "memory");
                    if (__all(v0 == tag && v1 == tag && v2 == tag && v3 == tag)) break;
                    __builtin_amdgcn_s_sleep(2);
                }
                if (l == 0) flag_store(p.ws, WS_EP, tag);
            }
            wait_word(FL_F2);
        }
        STAMP(12);
        f32x16 acc[2][2];
        for (int i = 0; i < 16; ++i) { acc[0][0][i] = 0.f; acc[0][1][i] = 0.f; acc[1][0][i] = 0.f; acc[1][1][i] = 0.f; }
        const unsigned hfo0 = WS_HF + (2 * rt + (xw >> 1)) * NUNIT * 1024 + l16, hfo1 = WS_HF + (2 * rt + ((xw >> 1) ^ 1)) * NUNIT * 1024 + l16;
        bf16x8 hq[22][2];
        auto h_load1 = [&](int i, int sh) {
            const unsigned so = (i == 21 && xw >= 2) ? WS_END : unit_of(i) * 1024;
            hq[i][sh] = __builtin_bit_cast(bf16x8, __builtin_amdgcn_raw_buffer_load_b128(wsr, sh ? hfo1 : hfo0, so, 16));
        };
#pragma unroll
        for (int i = 0; i < HAHEAD; ++i) { h_load1(i, 0); h_load1(i, 1); SB(); }
        bf16x8 wt[22][2];
        constexpr int TS = 18;
        static_assert(WD3 - 4 > HAHEAD && NWR - 1 > 0, "late Wd units must be covered by H loads consumed earlier");
#pragma unroll
        for (int i = 0; i < TS; ++i) {
            if (i == 1) STAMP(15);
            if (i == 9) STAMP(16);
#pragma unroll
            for (int m = 0; m < 4; ++m) {
                const int dh = m >> 1, sh = m & 1;
                const bf16x8 wv = i < NWR ? wf[i < NWR ? i : 0][dh] : wt[i][dh];
                acc[sh][dh] = __builtin_amdgcn_mfma_f32_32x32x16_bf16(wv, hq[i][sh], acc[sh][dh], 0, 0, 0);
                SB();
                if (m < 2 && i + 1 >= NWR) wt[i + 1][m] = wd_read(i + 1, m);
                if (m >= 2 && i >= TS - 3) wt[i + 4][m - 2] = wd_read(i + 4, m - 2);
                if (m >= 2 && i + HAHEAD < 22) h_load1(i + HAHEAD, m - 2);
                if (m == 0 && i == F3_AT) lflag[FL_PROG] = 1;
                if (m == 1 && WD3 + i < 22) wd_issue(WD3 + i, WD3 + i + 1);
                SB();
            }
        }
#pragma unroll
        for (int t = 3; t >= 0; --t) {
#pragma unroll
            for (int i = TS; i < 22; ++i) {
                acc[t >> 1][t & 1] = __builtin_amdgcn_mfma_f32_32x32x16_bf16(wt[i][t & 1], hq[i][t >> 1], acc[t >> 1][t & 1], 0, 0, 0);
                SB();
                if (TAILOV && t < 3 && i > TS) {
                    const f32x16 v = acc[(t + 1) >> 1][(t + 1) & 1];
                    const unsigned dst = (t == 2 ? wB : wA + t * 4096) + l16;
#pragma unroll
                    for (int a = (i == TS + 1 ? 0 : i - TS); a <= i - TS; ++a)
                        *(LAS f32x4*)(dst + a * 1024) = (f32x4){v[4 * a], v[4 * a + 1], v[4 * a + 2], v[4 * a + 3]};
                    SB();
                }
            }
        }
        STAMP(19);
        if (!TAILOV) {
#pragma unroll
            for (int t = 2; t >= 0; --t) {
                const f32x16 v = acc[(t + 1) >> 1][(t + 1) & 1];
                const unsigned dst = (t == 2 ? wB : wA + t * 4096) + l16;
#pragma unroll
                for (int a = 0; a < 4; ++a) *(LAS f32x4*)(dst + a * 1024) = (f32x4){v[4 * a], v[4 * a + 1], v[4 * a + 2], v[4 * a + 3]};
            }
        }
        asm volatile("" ::: "memory");
        lflag[FL_PART + xw] = 1;
        f32x16 own = acc[0][0];
        const int sl = 32 * (xw >> 1) + (l & 31), hi = l >> 5;
        const unsigned gaa = lds0 + L_RED + aw * 8192 + 2048 + hi * 1024 + sl * 16;
        const unsigned oo = ((64 * rt + sl) * D + 64 * c + 32 * aw + 8 * hi) * 2;
        STAMP(21);
        wait_word(FL_OMG);
        const float omg = *(LAS float*)(lds0 + L_OMG + sl * 4);
        {
            f32x4 t[12];
#pragma unroll 1
            for (int it = 0; it < SPIN_CAP; ++it) {
                const u32x4 f = *(volatile LAS u32x4*)(lds0 + L_FLAG + FL_PART * 4);
#pragma unroll
                for (int j = 0; j < 3; ++j) {
                    const int v = xw ^ (j + 1);
                    const unsigned src = (j == 2 ? lds0 + L_WB + v * 6144 : lds0 + L_WA + v * 10240 + j * 4096) + l16;
#pragma unroll
                    for (int a = 0; a < 4; ++a) t[4 * j + a] = *(volatile LAS f32x4*)(src + a * 1024);
                }
                if (__builtin_amdgcn_readfirstlane(f[0] & f[1] & f[2] & f[3])) break;
                __builtin_amdgcn_s_sleep(1);
            }
#pragma unroll
            for (int a = 0; a < 4; ++a) {
                const f32x4 u = (t[a] + t[4 + a]) + t[8 + a];
                for (int i = 0; i < 4; ++i) own[4 * a + i] = (own[4 * a + i] + u[i]) * omg;
            }
        }
        STAMP(22);
        {
            u32x4 ga[2];
#pragma unroll 1
            for (int it = 0; it < SPIN_CAP; ++it) {
                const u32x2 f = *(volatile LAS u32x2*)(lds0 + L_FLAG + (FL_GA + 2 * aw) * 4);
                ga[0] = *(volatile LAS u32x4*)(gaa + 4096); ga[1] = *(volatile LAS u32x4*)(gaa);
                if (__builtin_amdgcn_readfirstlane(f[0] & f[1] & 2)) break;
            }
            const __amdgpu_buffer_rsrc_t outr = __builtin_amdgcn_make_buffer_rsrc(p.out, 0, S * D * 2, 0x00020000);
#pragma unroll
            for (int r8 = 0; r8 < 2; ++r8) {
                unsigned o[4];
#pragma unroll
                for (int i = 0; i < 4; ++i) o[i] = pk_bf16(bf_lo(ga[r8][i]) + own[8 * r8 + 2 * i], bf_hi(ga[r8][i]) + own[8 * r8 + 2 * i + 1]);
                __builtin_amdgcn_raw_buffer_store_b128((u32x4){o[0], o[1], o[2], o[3]}, outr, oo + 32 * r8, 0, OUTAUX);
            }
        }
        asm volatile("fk_end_x:");
    }
}

extern "C" void kernel_launch(void* const* d_in, const int* in_sizes, int n_in,
                              void* d_out, int out_size, void* d_ws, size_t ws_size,
                              hipStream_t stream) {
    Params p;
    p.q = (const u16*)d_in[0]; p.k = (const u16*)d_in[1]; p.v = (const u16*)d_in[2];
    p.shift_k = (const u16*)d_in[3]; p.ssm_k = (const u16*)d_in[4]; p.ssm_D = (const u16*)d_in[5];
    p.wg = (const u16*)d_in[7]; p.wu = (const u16*)d_in[8]; p.wd = (const u16*)d_in[9]; p.gw = (const u16*)d_in[10];
    p.out = (u16*)d_out; p.ws = (char*)d_ws;
    (void)hipFuncSetAttribute((const void*)fused_kernel, hipFuncAttributeMaxDynamicSharedMemorySize, LDS_BYTES);
    fused_kernel<<<256, 512, LDS_BYTES, stream>>>(p);
}
```
